```python
import torch

def _softmax(x, axis=-1):
    return torch.softmax(x, dim=axis)
def _gelu(x):
    return torch.nn.functional.gelu(x, approximate="tanh")
def _silu(x):
    return torch.nn.functional.silu(x)
def _sigmoid(x):
    return torch.sigmoid(x)
def _layernorm(x, axis=-1, eps=1e-5):
    m = x.mean(dim=axis, keepdim=True)
    v = x.var(dim=axis, keepdim=True, unbiased=False)
    return (x - m) / torch.sqrt(v + eps)
def _scatter_add(tgt, idx, src, axis=0):
    return tgt.clone().index_add_(axis, idx, src)
def _segment_sum(src, seg, n):
    out = torch.zeros((n,) + src.shape[1:], dtype=src.dtype)
    return out.index_add_(0, seg, src)
def _pad(x, pw):
    flat = tuple(v for pair in reversed(pw) for v in pair)
    return torch.nn.functional.pad(x, flat)

kernel_name = 'standalone__mha_block_cached__near_ridge'

def setup_inputs():

    _w = (lambda a: (lambda t: t.to(torch.bfloat16) if t.is_floating_point() else t)(torch.as_tensor(a)))
    B, Sq, D = 8, 128, 448
    H, Sk, Dh = 16, 512, 256
    return {
        "x": _w((lambda *s: torch.randn(*s, dtype=torch.bfloat16) * 0.8)(B, Sq, D)),
        "k_cache": _w((lambda *s: torch.randn(*s, dtype=torch.bfloat16) * 0.8)(B, H, Sk, Dh)),
        "v_cache": _w((lambda *s: torch.randn(*s, dtype=torch.bfloat16) * 0.05)(B, H, Sk, Dh)),
        "wq": _w((lambda *s: torch.randn(*s, dtype=torch.bfloat16) * 0.8)(D, H * Dh) * (D ** -0.5)),
        "wo": _w((lambda *s: torch.randn(*s, dtype=torch.bfloat16) * 0.05)(H * Dh, D) * ((H * Dh) ** -0.5)),
    }

def reference(x, k_cache, v_cache, wq, wo):
    B, Sq, D = x.shape
    H, Dh = 16, 256
    q = (lambda x, *d: x.permute(*d))((x @ wq).reshape(B, Sq, H, Dh), 0, 2, 1, 3)
    scores = torch.einsum("bhqd,bhkd->bhqk", q, k_cache) * (Dh ** -0.5)
    p = _softmax(scores, axis=-1)
    o = torch.einsum("bhqk,bhkd->bhqd", p, v_cache)
    return (lambda x, *d: x.permute(*d))(o, 0, 2, 1, 3).reshape(B, Sq, H * Dh) @ wo
```

```cpp
#include <hip/hip_runtime.h>
#include <hip/hip_bf16.h>
#include <stdint.h>

typedef __attribute__((ext_vector_type(8))) short bf16x8;
typedef __attribute__((ext_vector_type(4))) short s16x4;
typedef __attribute__((ext_vector_type(16))) float f32x16;
typedef __attribute__((ext_vector_type(4))) float f32x4;
typedef __attribute__((ext_vector_type(2))) float f32x2;
typedef __attribute__((ext_vector_type(4))) unsigned u32x4;
typedef __attribute__((ext_vector_type(2))) unsigned u32x2;
typedef __attribute__((ext_vector_type(2))) __bf16 bf16x2_t;
typedef __attribute__((address_space(3))) s16x4* lds_s16x4_ptr;

#define DI __device__ __forceinline__
#define MFMA(a, b, c) __builtin_amdgcn_mfma_f32_32x32x16_bf16((a), (b), (c), 0, 0, 0)

constexpr int NB = 8, SQ = 128, DM = 448, NH = 16, SK = 512, DH = 256;
constexpr int HD = NH * DH;

DI unsigned cvtpk(float lo, float hi) {
  f32x2 v = {lo, hi};
  bf16x2_t r = __builtin_convertvector(v, bf16x2_t);
  return __builtin_bit_cast(unsigned, r);
}
DI void dma16(const char* sbase, unsigned voff, unsigned ldsa) {
  asm volatile("s_mov_b32 m0, %2\n\ts_nop 0\n\tglobal_load_lds_dwordx4 %0, %1" ::"v"(voff), "s"(sbase), "s"(ldsa) : "memory");
}
DI void dma16nt(const char* sbase, unsigned voff, unsigned ldsa) {
  asm volatile("s_mov_b32 m0, %2\n\ts_nop 0\n\tglobal_load_lds_dwordx4 %0, %1 nt" ::"v"(voff), "s"(sbase), "s"(ldsa) : "memory");
}
DI s16x4 tr_read(const char* l) {
  return __builtin_amdgcn_ds_read_tr16_b64_v4i16((lds_s16x4_ptr)(l));
}
DI bf16x8 cat8(s16x4 a, s16x4 b) { return __builtin_shufflevector(a, b, 0, 1, 2, 3, 4, 5, 6, 7); }
template <int N> DI void wait_bar() {
  asm volatile("s_waitcnt vmcnt(%0) lgkmcnt(0)\n\ts_barrier" ::"n"(N) : "memory");
}
template <int N> DI void wait_vm() {
  asm volatile("s_waitcnt vmcnt(%0) lgkmcnt(0)" ::"n"(N) : "memory");
}
DI float swap_half_max(float v) {
  auto rr = __builtin_amdgcn_permlane32_swap(__float_as_uint(v), __float_as_uint(v), false, false);
  return fmaxf(__uint_as_float(rr[0]), __uint_as_float(rr[1]));
}
DI float swap_half_sum(float v) {
  auto rr = __builtin_amdgcn_permlane32_swap(__float_as_uint(v), __float_as_uint(v), false, false);
  return __uint_as_float(rr[0]) + __uint_as_float(rr[1]);
}

constexpr int RSZ = 32768;
constexpr int K1_LDS = 5 * RSZ;
constexpr int XOFF = 3 * RSZ;
__host__ __device__ constexpr int stage_slot(int s) {
  return s < 7 ? (s % 3) : (((s - 7) % 5) == 0 ? 1 : ((s - 7) % 5) == 1 ? 2 : ((s - 7) % 5) == 2 ? 0 : ((s - 7) % 5) == 3 ? 4 : 3);
}
constexpr float SM_C = 0.0625f * 1.4426950408889634f;
constexpr float SM_THR = 8.0f / 0.0625f;

struct K1Ctx {
  char* lds;
  unsigned ldsa;
  const char* kg;
  const char* vg;
  const char* wg;
  int w, lane;
};

template <int S> DI void issue_stage(const K1Ctx& c) {
  constexpr int slot = stage_slot(S);
  const int hi = c.lane >> 5, l31 = c.lane & 31;
  const unsigned dst = c.ldsa + slot * RSZ + c.w * 8192;
  if constexpr (S < 7) {
    const char* src = c.wg + (size_t)(S * 64 + c.w * 16) * (HD * 2);
#pragma unroll
    for (int i = 0; i < 8; ++i) {
      const int sw = ((2 * i + hi) & 3) << 2;
      dma16(src + (size_t)i * 2 * (HD * 2), hi * (HD * 2) + ((l31 ^ sw) << 4), dst + i * 1024);
    }
  } else if constexpr (((S - 7) & 1) == 0) {
    const char* src = c.kg + ((S - 7) >> 1) * RSZ + c.w * 8192;
#pragma unroll
    for (int i = 0; i < 8; ++i) {
      const int sw = 2 * i + hi;
      dma16nt(src + i * 1024, hi * 512 + ((l31 ^ sw) << 4), dst + i * 1024);
    }
  } else {
    const char* src = c.vg + ((S - 7) >> 1) * RSZ + c.w * 8192;
#pragma unroll
    for (int i = 0; i < 8; ++i) {
      const int sw = ((2 * i + hi) & 3) << 2;
      dma16nt(src + i * 1024, hi * 512 + ((l31 ^ sw) << 4), dst + i * 1024);
    }
  }
}

template <int S> DI void qproj_stage(const K1Ctx& c, f32x16 (&qa)[2][2]) {
  constexpr int slot = stage_slot(S);
  const int lane = c.lane, h = lane >> 5, r = lane & 31, blk = (lane >> 4) & 1, qq = (lane & 15) >> 2, p = lane & 3;
  const char* wb = c.lds + slot * RSZ;
  const char* xb = c.lds + XOFF + S * 8192;
#pragma unroll
  for (int ks = 0; ks < 4; ++ks) {
    bf16x8 a[2], b[2];
#pragma unroll
    for (int dt = 0; dt < 2; ++dt) {
      const int t = 2 * c.w + dt;
      const char* ad = wb + (16 * ks + 8 * h + qq) * 512 + 64 * (t ^ qq) + 32 * blk + 8 * p;
      a[dt] = cat8(tr_read(ad), tr_read(ad + 4 * 512));
    }
#pragma unroll
    for (int qt = 0; qt < 2; ++qt) {
      const int q = 32 * qt + r;
      b[qt] = *(const bf16x8*)(xb + q * 128 + (((2 * ks + h) ^ ((q >> 1) & 7)) << 4));
    }
#pragma unroll
    for (int dt = 0; dt < 2; ++dt)
#pragma unroll
      for (int qt = 0; qt < 2; ++qt) qa[dt][qt] = MFMA(a[dt], b[qt], qa[dt][qt]);
  }
}

template <int T> DI void qk_softmax(const K1Ctx& c, const bf16x8 (&qf)[16], f32x16 (&o)[8], float& m, float& l, bf16x8 (&pf)[2]) {
  constexpr int slot = stage_slot(7 + 2 * T);
  const int lane = c.lane, h = lane >> 5, r = lane & 31, kh = c.w & 1;
  const int key = 32 * kh + r;
  const char* kb = c.lds + slot * RSZ + key * 512;
  f32x16 s;
#pragma unroll
  for (int i = 0; i < 16; ++i) s[i] = 0.f;
#pragma unroll
  for (int ks = 0; ks < 16; ++ks) {
    const bf16x8 a = *(const bf16x8*)(kb + (((2 * ks + h) ^ (key & 15)) << 4));
    s = MFMA(a, qf[ks], s);
  }
  float pmax = s[0];
#pragma unroll
  for (int i = 1; i < 16; ++i) pmax = fmaxf(pmax, s[i]);
  pmax = swap_half_max(pmax);
  if (__any(pmax - m > SM_THR)) {
    const float mn = fmaxf(m, pmax);
    const float alpha = __builtin_amdgcn_exp2f((m - mn) * SM_C);
    m = mn;
    l *= alpha;
#pragma unroll
    for (int t = 0; t < 8; ++t)
#pragma unroll
      for (int i = 0; i < 16; ++i) o[t][i] *= alpha;
  }
  const float mc = -m * SM_C;
  float ps = 0.f;
#pragma unroll
  for (int i = 0; i < 16; ++i) { s[i] = __builtin_amdgcn_exp2f(fmaf(s[i], SM_C, mc)); ps += s[i]; }
  l += ps;
#pragma unroll
  for (int ks = 0; ks < 2; ++ks) {
    u32x4 w = {cvtpk(s[8 * ks + 0], s[8 * ks + 1]), cvtpk(s[8 * ks + 2], s[8 * ks + 3]),
               cvtpk(s[8 * ks + 4], s[8 * ks + 5]), cvtpk(s[8 * ks + 6], s[8 * ks + 7])};
    pf[ks] = __builtin_bit_cast(bf16x8, w);
  }
}

template <int T> DI void pv(const K1Ctx& c, f32x16 (&o)[8], const bf16x8 (&pf)[2]) {
  constexpr int slot = stage_slot(8 + 2 * T);
  const int lane = c.lane, h = lane >> 5, blk = (lane >> 4) & 1, qq = (lane & 15) >> 2, p = lane & 3, kh = c.w & 1;
  const char* vb = c.lds + slot * RSZ + (32 * kh + 4 * h + qq) * 512 + 32 * blk + 8 * p;
#pragma unroll
  for (int t = 0; t < 8; ++t) {
#pragma unroll
    for (int ks = 0; ks < 2; ++ks) {
      const char* ad = vb + (16 * ks) * 512 + 64 * (t ^ qq);
      const bf16x8 a = cat8(tr_read(ad), tr_read(ad + 8 * 512));
      o[t] = MFMA(a, pf[ks], o[t]);
    }
  }
}

template <int T> DI void attn_tile(const K1Ctx& c, const bf16x8 (&qf)[16], f32x16 (&o)[8], float& m, float& l) {
  constexpr int sK = 7 + 2 * T, sV = 8 + 2 * T;
  bf16x8 pf[2];
  if constexpr (T > 0) {
    wait_bar<(sK <= 19 ? 24 : (22 - sK) * 8)>();
    if constexpr (sK + 4 <= 22) issue_stage<sK + 4>(c);
  }
  qk_softmax<T>(c, qf, o, m, l, pf);
  wait_bar<(sV == 8 ? 16 : sV <= 19 ? 24 : (22 - sV) * 8)>();
  if constexpr (sV == 8) issue_stage<11>(c);
  if constexpr (sV + 4 <= 22) issue_stage<sV + 4>(c);
  pv<T>(c, o, pf);
}

__global__ __launch_bounds__(256, 1) void mha_attn_kernel(const __hip_bfloat16* __restrict__ x, const __hip_bfloat16* __restrict__ kc,
                                                          const __hip_bfloat16* __restrict__ vc, const __hip_bfloat16* __restrict__ wq,
                                                          __hip_bfloat16* __restrict__ ows) {
  extern __shared__ __attribute__((aligned(16))) char lds[];
  const int tid = threadIdx.x, lane = tid & 63;
  const int w = __builtin_amdgcn_readfirstlane(tid >> 6);
  const int bid = blockIdx.x;
  const int xcd = bid & 7, idx = bid >> 3;
  const int head = xcd * 2 + (idx & 1), qh = (idx >> 1) & 1, b = idx >> 2;

  K1Ctx c;
  c.lds = lds; c.ldsa = (unsigned)(uintptr_t)lds; c.w = w; c.lane = lane;
  c.kg = (const char*)kc + (size_t)(b * NH + head) * SK * DH * 2;
  c.vg = (const char*)vc + (size_t)(b * NH + head) * SK * DH * 2;
  c.wg = (const char*)wq + head * DH * 2;

  {
    const char* xg = (const char*)x + (size_t)(b * SQ + qh * 64) * (DM * 2);
#pragma unroll
    for (int ck = 0; ck < 7; ++ck)
#pragma unroll
      for (int i = 0; i < 2; ++i) {
        const int row = (w * 2 + i) * 8 + (lane >> 3);
        const int ch = (lane & 7) ^ ((row >> 1) & 7);
        dma16(xg + ck * 128, row * (DM * 2) + ch * 16, c.ldsa + XOFF + ck * 8192 + (w * 2 + i) * 1024);
      }
  }
  issue_stage<0>(c); issue_stage<1>(c); issue_stage<2>(c);

  f32x16 qa[2][2];
#pragma unroll
  for (int i = 0; i < 2; ++i)
#pragma unroll
    for (int j = 0; j < 2; ++j)
#pragma unroll
      for (int e = 0; e < 16; ++e) qa[i][j][e] = 0.f;
  wait_bar<16>(); qproj_stage<0>(c, qa);
  wait_bar<8>(); issue_stage<3>(c); qproj_stage<1>(c, qa);
  wait_bar<8>(); issue_stage<4>(c); qproj_stage<2>(c, qa);
  wait_bar<8>(); issue_stage<5>(c); qproj_stage<3>(c, qa);
  wait_bar<8>(); issue_stage<6>(c); qproj_stage<4>(c, qa);
  wait_bar<8>(); issue_stage<7>(c); qproj_stage<5>(c, qa);
  wait_bar<8>(); issue_stage<8>(c); qproj_stage<6>(c, qa);
  wait_bar<16>();
  issue_stage<9>(c); issue_stage<10>(c);
  {
    const int h = lane >> 5, r = lane & 31;
    char* qi = lds + 3 * RSZ;
#pragma unroll
    for (int dt = 0; dt < 2; ++dt)
#pragma unroll
      for (int qt = 0; qt < 2; ++qt) {
        const int q = 32 * qt + r;
#pragma unroll
        for (int g = 0; g < 4; ++g) {
          const int ch = 8 * w + 4 * dt + g;
          u32x2 v = {cvtpk(qa[dt][qt][4 * g + 0], qa[dt][qt][4 * g + 1]), cvtpk(qa[dt][qt][4 * g + 2], qa[dt][qt][4 * g + 3])};
          *(u32x2*)(qi + q * 512 + ((ch ^ (q & 15)) << 4) + 8 * h) = v;
        }
      }
  }
  wait_bar<24>();
  bf16x8 qf[16];
  {
    const int h = lane >> 5, r = lane & 31, q = 32 * (w >> 1) + r;
    const char* qi = lds + 3 * RSZ + q * 512;
#pragma unroll
    for (int ks = 0; ks < 16; ++ks) qf[ks] = *(const bf16x8*)(qi + (((2 * ks + h) ^ (q & 15)) << 4));
  }

  f32x16 o[8];
#pragma unroll
  for (int t = 0; t < 8; ++t)
#pragma unroll
    for (int e = 0; e < 16; ++e) o[t][e] = 0.f;
  float m = -1e30f, l = 0.f;
  attn_tile<0>(c, qf, o, m, l);
  attn_tile<1>(c, qf, o, m, l);
  attn_tile<2>(c, qf, o, m, l);
  attn_tile<3>(c, qf, o, m, l);
  attn_tile<4>(c, qf, o, m, l);
  attn_tile<5>(c, qf, o, m, l);
  attn_tile<6>(c, qf, o, m, l);
  attn_tile<7>(c, qf, o, m, l);

  {
    const int h = lane >> 5, r = lane & 31, kh = w & 1;
    l = swap_half_sum(l);
    char* ex = lds + 2 * RSZ;
    float* st = (float*)(lds + 4 * RSZ);
    st[w * 128 + lane] = m;
    st[w * 128 + 64 + lane] = l;
    {
      char* dst = ex + w * 16384 + lane * 16;
#pragma unroll
      for (int t = 0; t < 4; ++t)
#pragma unroll
        for (int g = 0; g < 4; ++g) {
          f32x4 v;
          if (kh == 0) v = f32x4{o[4 + t][4 * g], o[4 + t][4 * g + 1], o[4 + t][4 * g + 2], o[4 + t][4 * g + 3]};
          else v = f32x4{o[t][4 * g], o[t][4 * g + 1], o[t][4 * g + 2], o[t][4 * g + 3]};
          *(f32x4*)(dst + (t * 4 + g) * 1024) = v;
        }
    }
    wait_bar<0>();
    const int pw = w ^ 1;
    const float m2 = st[pw * 128 + lane], l2 = st[pw * 128 + 64 + lane];
    const float mn = fmaxf(m, m2);
    const float a1 = __builtin_amdgcn_exp2f((m - mn) * SM_C), a2 = __builtin_amdgcn_exp2f((m2 - mn) * SM_C);
    const float inv = 1.0f / (l * a1 + l2 * a2);
    const float f1 = a1 * inv, f2 = a2 * inv;
    const char* src = ex + pw * 16384 + lane * 16;
    char* oi = lds;
    const int q = 32 * (w >> 1) + r;
#pragma unroll
    for (int t = 0; t < 4; ++t)
#pragma unroll
      for (int g = 0; g < 4; ++g) {
        const f32x4 v = *(const f32x4*)(src + (t * 4 + g) * 1024);
        float r0, r1, r2, r3;
        if (kh == 0) { r0 = o[t][4 * g] * f1 + v[0] * f2; r1 = o[t][4 * g + 1] * f1 + v[1] * f2; r2 = o[t][4 * g + 2] * f1 + v[2] * f2; r3 = o[t][4 * g + 3] * f1 + v[3] * f2; }
        else { r0 = o[4 + t][4 * g] * f1 + v[0] * f2; r1 = o[4 + t][4 * g + 1] * f1 + v[1] * f2; r2 = o[4 + t][4 * g + 2] * f1 + v[2] * f2; r3 = o[4 + t][4 * g + 3] * f1 + v[3] * f2; }
        const int ch = 4 * (4 * kh + t) + g;
        u32x2 pk = {cvtpk(r0, r1), cvtpk(r2, r3)};
        *(u32x2*)(oi + q * 512 + ((ch ^ (q & 15)) << 4) + 8 * h) = pk;
      }
    wait_bar<0>();
    char* og = (char*)ows + (size_t)(b * SQ + qh * 64) * (HD * 2) + head * (DH * 2);
#pragma unroll
    for (int i = 0; i < 8; ++i) {
      const int row = i * 8 + (tid >> 5), ch = tid & 31;
      const u32x4 v = *(const u32x4*)(oi + row * 512 + ((ch ^ (row & 15)) << 4));
      *(u32x4*)(og + (size_t)row * (HD * 2) + ch * 16) = v;
    }
  }
}

constexpr int K2_STG = 12288;
constexpr int K2_LDS = 4 * 3 * K2_STG;

DI void k2_issue(const char* ag, const char* bg, unsigned dst, int s, int lane) {
#pragma unroll
  for (int i = 0; i < 4; ++i) {
    const int row = i * 8 + (lane >> 3);
    const int ch = (lane & 7) ^ ((row >> 1) & 7);
    dma16(ag + s * 128, row * (HD * 2) + ch * 16, dst + i * 1024);
  }
#pragma unroll
  for (int i = 0; i < 8; ++i) {
    const int row = i * 8 + (lane >> 3);
    const int ch = (lane & 7) ^ (((row >> 1) & 1) << 2);
    dma16(bg + (size_t)(s * 64) * (DM * 2), row * (DM * 2) + ch * 16, dst + 4096 + i * 1024);
  }
}

__global__ __launch_bounds__(256, 1) void mha_oproj_kernel(const __hip_bfloat16* __restrict__ ows, const __hip_bfloat16* __restrict__ wo,
                                                           __hip_bfloat16* __restrict__ out) {
  extern __shared__ __attribute__((aligned(16))) char lds[];
  const int tid = threadIdx.x, lane = tid & 63;
  const int w = __builtin_amdgcn_readfirstlane(tid >> 6);
  const int bid = blockIdx.x;
  const int xcd = bid & 7, j = bid >> 3;
  const int mt = xcd * 4 + j / 7, nt = j % 7;
  const char* ag = (const char*)ows + (size_t)(mt * 32) * (HD * 2) + w * 2048;
  const char* bg = (const char*)wo + (size_t)(w * 1024) * (DM * 2) + nt * 128;
  char* ring = lds + w * 3 * K2_STG;
  const unsigned ringa = (unsigned)(uintptr_t)lds + w * 3 * K2_STG;
  const int h = lane >> 5, r = lane & 31, blk = (lane >> 4) & 1, qq = (lane & 15) >> 2, p = lane & 3;

  f32x16 acc[2];
#pragma unroll
  for (int i = 0; i < 2; ++i)
#pragma unroll
    for (int e = 0; e < 16; ++e) acc[i][e] = 0.f;
  k2_issue(ag, bg, ringa, 0, lane);
  k2_issue(ag, bg, ringa + K2_STG, 1, lane);
  int slot = 0;
  for (int s = 0; s < 16; ++s) {
    if (s < 15) wait_vm<12>(); else wait_vm<0>();
    if (s + 2 < 16) { const int ns = slot == 0 ? 2 : slot - 1; k2_issue(ag, bg, ringa + ns * K2_STG, s + 2, lane); }
    const char* ab = ring + slot * K2_STG;
    const char* bb = ab + 4096;
#pragma unroll
    for (int ks = 0; ks < 4; ++ks) {
      const bf16x8 a = *(const bf16x8*)(ab + r * 128 + (((2 * ks + h) ^ ((r >> 1) & 7)) << 4));
#pragma unroll
      for (int n2 = 0; n2 < 2; ++n2) {
        const char* ad = bb + (16 * ks + 8 * h + qq) * 128 + 64 * (n2 ^ ((qq >> 1) & 1)) + 32 * blk + 8 * p;
        const bf16x8 bf = cat8(tr_read(ad), tr_read(ad + 4 * 128));
        acc[n2] = MFMA(a, bf, acc[n2]);
      }
    }
    slot = slot == 2 ? 0 : slot + 1;
  }
  wait_bar<0>();
  float* red = (float*)lds;
#pragma unroll
  for (int n2 = 0; n2 < 2; ++n2)
#pragma unroll
    for (int e = 0; e < 16; ++e) {
      const int mrow = (e & 3) + 8 * (e >> 2) + 4 * h;
      red[w * 2048 + mrow * 64 + n2 * 32 + r] = acc[n2][e];
    }
  wait_bar<0>();
  {
    const int mrow = tid >> 3, n0 = (tid & 7) * 8;
    f32x4 s0 = {0.f, 0.f, 0.f, 0.f}, s1 = {0.f, 0.f, 0.f, 0.f};
#pragma unroll
    for (int ww = 0; ww < 4; ++ww) {
      s0 += *(const f32x4*)(red + ww * 2048 + mrow * 64 + n0);
      s1 += *(const f32x4*)(red + ww * 2048 + mrow * 64 + n0 + 4);
    }
    u32x4 pk = {cvtpk(s0[0], s0[1]), cvtpk(s0[2], s0[3]), cvtpk(s1[0], s1[1]), cvtpk(s1[2], s1[3])};
    *(u32x4*)((char*)out + (size_t)(mt * 32 + mrow) * (DM * 2) + nt * 128 + n0 * 2) = pk;
  }
}

extern "C" void kernel_launch(void* const* d_in, const int* in_sizes, int n_in, void* d_out, int out_size, void* d_ws, size_t ws_size,
                              hipStream_t stream) {
  const __hip_bfloat16* x = (const __hip_bfloat16*)d_in[0];
  const __hip_bfloat16* kc = (const __hip_bfloat16*)d_in[1];
  const __hip_bfloat16* vc = (const __hip_bfloat16*)d_in[2];
  const __hip_bfloat16* wq = (const __hip_bfloat16*)d_in[3];
  const __hip_bfloat16* wo = (const __hip_bfloat16*)d_in[4];
  __hip_bfloat16* out = (__hip_bfloat16*)d_out;
  __hip_bfloat16* ows = (__hip_bfloat16*)d_ws;
  (void)hipFuncSetAttribute((const void*)mha_attn_kernel, hipFuncAttributeMaxDynamicSharedMemorySize, K1_LDS);
  (void)hipFuncSetAttribute((const void*)mha_oproj_kernel, hipFuncAttributeMaxDynamicSharedMemorySize, K2_LDS);
  mha_attn_kernel<<<256, 256, K1_LDS, stream>>>(x, kc, vc, wq, ows);
  mha_oproj_kernel<<<224, 256, K2_LDS, stream>>>(ows, wo, out);
}
```

```cpp
#include <hip/hip_runtime.h>
#include <hip/hip_bf16.h>
#include <stdint.h>
#include <utility>

typedef __attribute__((ext_vector_type(8))) short bf16x8;
typedef __attribute__((ext_vector_type(4))) short s16x4;
typedef __attribute__((ext_vector_type(16))) float f32x16;
typedef __attribute__((ext_vector_type(4))) float f32x4;
typedef __attribute__((ext_vector_type(2))) float f32x2;
typedef __attribute__((ext_vector_type(4))) unsigned u32x4;
typedef __attribute__((ext_vector_type(2))) unsigned u32x2;
typedef __attribute__((ext_vector_type(2))) __bf16 bf16x2_t;
typedef __attribute__((address_space(3))) s16x4* lds_s16x4_ptr;

#define DI __device__ __forceinline__
#define MFMA(a, b, c) __builtin_amdgcn_mfma_f32_32x32x16_bf16((a), (b), (c), 0, 0, 0)
#define GAP() __builtin_amdgcn_sched_barrier(0)
#define ACLOB "a0","a1","a2","a3","a4","a5","a6","a7","a8","a9","a10","a11","a12","a13","a14","a15","a16","a17","a18","a19","a20","a21","a22","a23","a24","a25","a26","a27","a28","a29","a30","a31","a32","a33","a34","a35","a36","a37","a38","a39","a40","a41","a42","a43","a44","a45","a46","a47","a48","a49","a50","a51","a52","a53","a54","a55","a56","a57","a58","a59","a60","a61","a62","a63","a64","a65","a66","a67","a68","a69","a70","a71","a72","a73","a74","a75","a76","a77","a78","a79","a80","a81","a82","a83","a84","a85","a86","a87","a88","a89","a90","a91","a92","a93","a94","a95","a96","a97","a98","a99","a100","a101","a102","a103","a104","a105","a106","a107","a108","a109","a110","a111","a112","a113","a114","a115","a116","a117","a118","a119","a120","a121","a122","a123","a124","a125","a126","a127","a128","a129","a130","a131","a132","a133","a134","a135","a136","a137","a138","a139","a140","a141","a142","a143","a144","a145","a146","a147","a148","a149","a150","a151","a152","a153","a154","a155","a156","a157","a158","a159","a160","a161","a162","a163","a164","a165","a166","a167","a168","a169","a170","a171","a172","a173","a174","a175","a176","a177","a178","a179","a180","a181","a182","a183","a184","a185","a186","a187","a188","a189","a190","a191","a192","a193","a194","a195","a196","a197","a198","a199","a200","a201","a202","a203","a204","a205","a206","a207","a208","a209","a210","a211","a212","a213","a214","a215","a216","a217","a218","a219","a220","a221","a222","a223","a224","a225","a226","a227","a228","a229","a230","a231","a232","a233","a234","a235","a236","a237","a238","a239","a240","a241","a242","a243","a244","a245","a246","a247","a248","a249","a250","a251","a252","a253","a254","a255"
template <int... Is> using iseq = std::integer_sequence<int, Is...>;
template <int N> using make_iseq = std::make_integer_sequence<int, N>;

constexpr int NB = 8, SQ = 128, DM = 448, NH = 16, SK = 512, DH = 256;
constexpr int HD = NH * DH;

DI unsigned cvtpk(float lo, float hi) {
  f32x2 v = {lo, hi};
  bf16x2_t r = __builtin_convertvector(v, bf16x2_t);
  return __builtin_bit_cast(unsigned, r);
}
DI void dma16(const char* sbase, unsigned voff, unsigned ldsa) {
  asm volatile("s_mov_b32 m0, %2\n\ts_nop 0\n\tglobal_load_lds_dwordx4 %0, %1" ::"v"(voff), "s"(sbase), "s"(ldsa) : "memory");
}
DI s16x4 tr_read(const char* l) {
  return __builtin_amdgcn_ds_read_tr16_b64_v4i16((lds_s16x4_ptr)(l));
}
DI bf16x8 cat8(s16x4 a, s16x4 b) { return __builtin_shufflevector(a, b, 0, 1, 2, 3, 4, 5, 6, 7); }
template <int N> DI void wait_bar() {
  asm volatile("s_waitcnt vmcnt(%0) lgkmcnt(0)\n\ts_barrier" ::"n"(N) : "memory");
}
template <int N> DI void wait_vm() {
  asm volatile("s_waitcnt vmcnt(%0) lgkmcnt(0)" ::"n"(N) : "memory");
}
DI float swap_half_max(float v) {
  auto rr = __builtin_amdgcn_permlane32_swap(__float_as_uint(v), __float_as_uint(v), false, false);
  return fmaxf(__uint_as_float(rr[0]), __uint_as_float(rr[1]));
}
DI float swap_half_sum(float v) {
  auto rr = __builtin_amdgcn_permlane32_swap(__float_as_uint(v), __float_as_uint(v), false, false);
  return __uint_as_float(rr[0]) + __uint_as_float(rr[1]);
}
DI float max3f(float a, float b, float c) { float r; asm("v_max3_f32 %0, %1, %2, %3" : "=v"(r) : "v"(a), "v"(b), "v"(c)); return r; }
DI void mfma_pad() { asm volatile("s_nop 7\n\ts_nop 7" ::: "memory"); }

template <int A, int OFF> DI void rd128(unsigned addr) {
  asm volatile("ds_read_b128 a[%c1:%c2], %0 offset:%c3" ::"v"(addr), "i"(A), "i"(A + 3), "i"(OFF) : "memory", ACLOB);
}
template <int A, int OFF> DI void rdtr(unsigned addr) {
  asm volatile("ds_read_b64_tr_b16 a[%c1:%c2], %0 offset:%c3" ::"v"(addr), "i"(A), "i"(A + 1), "i"(OFF) : "memory", ACLOB);
}
template <int D, int A, int B, bool ZERO> DI void mfma_aaa() {
  if constexpr (ZERO) asm volatile("v_mfma_f32_32x32x16_bf16 a[%c0:%c1], a[%c2:%c3], a[%c4:%c5], 0" ::"i"(D), "i"(D + 15), "i"(A), "i"(A + 3), "i"(B), "i"(B + 3) : ACLOB);
  else asm volatile("v_mfma_f32_32x32x16_bf16 a[%c0:%c1], a[%c2:%c3], a[%c4:%c5], a[%c0:%c1]" ::"i"(D), "i"(D + 15), "i"(A), "i"(A + 3), "i"(B), "i"(B + 3) : ACLOB);
}
template <int A, int B, bool ZERO> DI void mfma_vaa(f32x16& s) {
  if constexpr (ZERO) asm volatile("v_mfma_f32_32x32x16_bf16 %0, a[%c1:%c2], a[%c3:%c4], 0" : "=v"(s) : "i"(A), "i"(A + 3), "i"(B), "i"(B + 3) : ACLOB);
  else asm volatile("v_mfma_f32_32x32x16_bf16 %0, a[%c1:%c2], a[%c3:%c4], %0" : "+v"(s) : "i"(A), "i"(A + 3), "i"(B), "i"(B + 3) : ACLOB);
}
template <int D, int A, bool ZERO> DI void mfma_aav(const bf16x8& p) {
  if constexpr (ZERO) asm volatile("v_mfma_f32_32x32x16_bf16 a[%c1:%c2], a[%c3:%c4], %0, 0" ::"v"(p), "i"(D), "i"(D + 15), "i"(A), "i"(A + 3) : ACLOB);
  else asm volatile("v_mfma_f32_32x32x16_bf16 a[%c1:%c2], a[%c3:%c4], %0, a[%c1:%c2]" ::"v"(p), "i"(D), "i"(D + 15), "i"(A), "i"(A + 3) : ACLOB);
}
template <int A> DI float ard() { float r; asm volatile("v_accvgpr_read_b32 %0, a[%c1]" : "=v"(r) : "i"(A)); return r; }
template <int A> DI void ascale1(float f) {
  float t;
  asm volatile("v_accvgpr_read_b32 %0, a[%c2]\n\tv_mul_f32 %0, %1, %0\n\tv_accvgpr_write_b32 a[%c2], %0" : "=&v"(t) : "v"(f), "i"(A) : ACLOB);
}
template <int... Is> DI void ascale_all(float f, iseq<Is...>) { (ascale1<Is>(f), ...); }

constexpr int RSZ = 32768;
constexpr int K1_LDS = 5 * RSZ;
constexpr int XOFF = 3 * RSZ;
constexpr int AQ = 128, AF = 192;
__host__ __device__ constexpr int stage_slot(int s) {
  return s < 7 ? (s % 3) : s == 7 ? 1 : s == 8 ? 2 : s == 9 ? 0 : s == 10 ? 4
       : (((s - 11) % 5) == 0 ? 1 : ((s - 11) % 5) == 1 ? 3 : ((s - 11) % 5) == 2 ? 2 : ((s - 11) % 5) == 3 ? 0 : 4);
}
__host__ __device__ constexpr bool stage_is_k(int s) { return s == 7 || (s >= 8 && s <= 20 && (s & 1) == 0); }
__host__ __device__ constexpr int stage_tile(int s) { return s == 7 ? 0 : s == 22 ? 7 : (s & 1) == 0 ? (s - 6) / 2 : (s - 9) / 2; }
constexpr float SM_C = 0.0625f * 1.4426950408889634f;
constexpr float SM_THR = 8.0f / 0.0625f;

struct K1Ctx {
  char* lds;
  unsigned ldsa;
  const char* kg;
  const char* vg;
  const char* wg;
  int w, lane;
  unsigned kaddr[8], vaddr[4], waddr[2], xaddr[4];
  f32x16 S[2];
  bf16x8 P[2], P2[2];
  float m, l, alpha, mc, ps0, ps1, pm;
  bool pend;
};

template <int S, int I> DI void issue_piece(const K1Ctx& c) {
  constexpr int slot = stage_slot(S);
  const int hi = c.lane >> 5, l31 = c.lane & 31;
  const unsigned dst = c.ldsa + slot * RSZ + c.w * 8192 + I * 1024;
  if constexpr (S < 7) {
    const char* src = c.wg + (size_t)(S * 64 + c.w * 16) * (HD * 2);
    const int sw = ((2 * I + hi) & 3) << 2;
    dma16(src + (size_t)I * 2 * (HD * 2), hi * (HD * 2) + ((l31 ^ sw) << 4), dst);
  } else if constexpr (stage_is_k(S)) {
    const char* src = c.kg + stage_tile(S) * RSZ + c.w * 8192;
    const int sw = 2 * I + hi;
    dma16(src + I * 1024, hi * 512 + ((l31 ^ sw) << 4), dst);
  } else {
    const char* src = c.vg + stage_tile(S) * RSZ + c.w * 8192;
    const int sw = ((2 * I + hi) & 3) << 2;
    dma16(src + I * 1024, hi * 512 + ((l31 ^ sw) << 4), dst);
  }
}
template <int S, int... Is> DI void issue_stage_(const K1Ctx& c, iseq<Is...>) { (issue_piece<S, Is>(c), ...); }
template <int S> DI void issue_stage(const K1Ctx& c) { issue_stage_<S>(c, make_iseq<8>{}); }

template <int S, int J> DI void kfrag(const K1Ctx& c) {
  rd128<AF + 4 * J, 256 * (J >> 3)>(c.kaddr[J & 7] + stage_slot(S) * RSZ);
}
template <int S, int J> DI void vfrag(const K1Ctx& c) {
  constexpr int t = J >> 1, ks = J & 1;
  rdtr<AF + 4 * J, ks * 8192 + 256 * (t >> 2)>(c.vaddr[t & 3] + stage_slot(S) * RSZ);
  rdtr<AF + 4 * J + 2, ks * 8192 + 256 * (t >> 2) + 4096>(c.vaddr[t & 3] + stage_slot(S) * RSZ);
}
__host__ __device__ constexpr int qset(int s) { return (s & 1) ? AF : AQ; }
template <int S, int J> DI void wfrag(const K1Ctx& c) {
  if constexpr (J < 8) {
    constexpr int ks = J >> 1, dt = J & 1;
    rdtr<qset(S) + 4 * J, ks * 8192>(c.waddr[dt] + stage_slot(S) * RSZ);
    rdtr<qset(S) + 4 * J + 2, ks * 8192 + 2048>(c.waddr[dt] + stage_slot(S) * RSZ);
  } else {
    constexpr int ks = (J - 8) >> 1, qt = (J - 8) & 1;
    rd128<qset(S) + 4 * J, qt * 4096>(c.xaddr[ks] + S * 8192);
  }
}
__host__ __device__ constexpr int worder(int g) { return (g & 3) < 2 ? 2 * (g >> 2) + (g & 1) : 8 + 2 * (g >> 2) + (g & 1); }

template <int P, int G> DI void qp_gap(K1Ctx& c) {
  constexpr int S = P - 1, ks = G >> 2, dt = (G >> 1) & 1, qt = G & 1;
  mfma_aaa<16 * (2 * dt + qt), qset(S) + 4 * (2 * ks + dt), qset(S) + 32 + 4 * (2 * ks + qt), (S == 0 && ks == 0)>();
  if constexpr (P < 7) wfrag<P, worder(G)>(c); else kfrag<7, G>(c);
  if constexpr (G < 8) issue_piece<P + 2, G>(c);
  else if constexpr (P == 7) issue_piece<10, G - 8>(c);
  GAP();
}
template <int P, int... Gs> DI void qp_phase_(K1Ctx& c, iseq<Gs...>) { (qp_gap<P, Gs>(c), ...); }
template <int P> DI void qp_phase(K1Ctx& c) { wait_bar<8>(); GAP(); qp_phase_<P>(c, make_iseq<16>{}); }
template <int... Gs> DI void wfrag0_(const K1Ctx& c, iseq<Gs...>) { (wfrag<0, worder(Gs)>(c), ...); }

template <int X, int E> DI void sm_exp(K1Ctx& c) {
  const float p = __builtin_amdgcn_exp2f(fmaf(c.S[X][E], SM_C, c.mc));
  c.S[X][E] = p;
}
template <int X, int E> DI void sm_add(K1Ctx& c) { if constexpr (E & 1) c.ps1 += c.S[X][E]; else c.ps0 += c.S[X][E]; }
template <int X, int KS, bool LAST = false> DI void sm_pack(K1Ctx& c) {
  u32x4 w = {cvtpk(c.S[X][8 * KS + 0], c.S[X][8 * KS + 1]), cvtpk(c.S[X][8 * KS + 2], c.S[X][8 * KS + 3]),
             cvtpk(c.S[X][8 * KS + 4], c.S[X][8 * KS + 5]), cvtpk(c.S[X][8 * KS + 6], c.S[X][8 * KS + 7])};
  if constexpr (LAST) c.P2[KS] = __builtin_bit_cast(bf16x8, w); else c.P[KS] = __builtin_bit_cast(bf16x8, w);
}
template <int T> DI void sm_decide(K1Ctx& c) {
  const float pmax = swap_half_max(c.pm);
  if constexpr (T == 0) {
    c.m = pmax;
  } else {
    if (__builtin_expect(__any(pmax - c.m > SM_THR), 0)) {
      const float mn = fmaxf(c.m, pmax);
      c.alpha = __builtin_amdgcn_exp2f((c.m - mn) * SM_C);
      c.m = mn;
      c.l *= c.alpha;
      c.pend = true;
    }
  }
  c.mc = -c.m * SM_C;
  c.ps0 = 0.f; c.ps1 = 0.f;
}

template <int T, int G> DI void a_gap(K1Ctx& c) {
  constexpr int X = T & 1, Xp = X ^ 1, PG = 8 + 2 * T;
  mfma_vaa<AF + 4 * G, AQ + 4 * G, (G == 0)>(c.S[X]);
  if constexpr (T > 0) {
    vfrag<PG - 1, G>(c);
    if constexpr (G < 8) { sm_exp<Xp, 8 + G>(c); if constexpr (G > 0) sm_add<Xp, 7 + G>(c); }
    if constexpr (G == 8) { sm_add<Xp, 15>(c); sm_pack<Xp, 0>(c); }
    if constexpr (G == 9) { sm_pack<Xp, 1>(c); c.l += c.ps0 + c.ps1; }
  }
  if constexpr (G >= 6 && G < 14 && PG + 3 <= 22) issue_piece<PG + 3, G - 6>(c);
  GAP();
}
template <int T, int... Gs> DI void a_phase_(K1Ctx& c, iseq<Gs...>) { (a_gap<T, Gs>(c), ...); }
template <int T> DI void a_phase(K1Ctx& c) {
  constexpr int PG = 8 + 2 * T, need = PG - 1;
  if constexpr (T > 0) { wait_bar<(need + 3 <= 22 ? 24 : (22 - need) * 8)>(); GAP(); }
  a_phase_<T>(c, make_iseq<16>{});
}
template <int T, int G> DI void b_gap(K1Ctx& c) {
  constexpr int X = T & 1, PG = 9 + 2 * T;
  if constexpr (T > 0) mfma_aav<16 * (G >> 1), AF + 4 * G, (T == 1 && (G & 1) == 0)>(c.P[G & 1]);
  if constexpr (T < 7) kfrag<PG - 1, G>(c); else vfrag<22, G>(c);
  if constexpr (G == 0) { c.pm = max3f(c.S[X][0], c.S[X][1], c.S[X][2]); c.pm = max3f(c.pm, c.S[X][3], c.S[X][4]); c.pm = max3f(c.pm, c.S[X][5], c.S[X][6]); }
  if constexpr (G == 1) { c.pm = max3f(c.pm, c.S[X][7], c.S[X][8]); c.pm = max3f(c.pm, c.S[X][9], c.S[X][10]); c.pm = max3f(c.pm, c.S[X][11], c.S[X][12]); }
  if constexpr (G == 2) { c.pm = max3f(c.pm, c.S[X][13], c.S[X][14]); c.pm = fmaxf(c.pm, c.S[X][15]); }
  if constexpr (G == 3) sm_decide<T>(c);
  if constexpr (T < 7) {
    if constexpr (G >= 4 && G < 12) { sm_exp<X, G - 4>(c); if constexpr (G > 4) sm_add<X, G - 5>(c); }
    if constexpr (G == 12) sm_add<X, 7>(c);
  } else {
    if constexpr (G >= 4 && G < 12) { sm_exp<X, 2 * (G - 4)>(c); sm_exp<X, 2 * (G - 4) + 1>(c); if constexpr (G > 4) { sm_add<X, 2 * (G - 5)>(c); sm_add<X, 2 * (G - 5) + 1>(c); } }
    if constexpr (G == 12) { sm_add<X, 14>(c); sm_add<X, 15>(c); sm_pack<X, 0, true>(c); }
    if constexpr (G == 13) { sm_pack<X, 1, true>(c); c.l += c.ps0 + c.ps1; }
  }
  if constexpr (G >= 6 && G < 14 && PG + 3 <= 22) issue_piece<PG + 3, G - 6>(c);
  GAP();
}
template <int T, int... Gs> DI void b_phase_(K1Ctx& c, iseq<Gs...>) { (b_gap<T, Gs>(c), ...); }
template <int T> DI void b_phase(K1Ctx& c) {
  constexpr int PG = 9 + 2 * T, need = PG - 1;
  mfma_pad();
  wait_bar<(need + 3 <= 22 ? 24 : (22 - need) * 8)>(); GAP();
  b_phase_<T>(c, make_iseq<16>{});
  if constexpr (T > 0) {
    if (__builtin_expect(c.pend, 0)) {
      if constexpr (T > 1) { mfma_pad(); ascale_all(c.alpha, make_iseq<128>{}); asm volatile("s_nop 3"); }
      else { mfma_pad(); ascale_all(c.alpha, make_iseq<128>{}); asm volatile("s_nop 3"); }
      c.pend = false;
    }
  }
}
template <int G> DI void pv_last_gap(K1Ctx& c) { mfma_aav<16 * (G >> 1), AF + 4 * G, false>(c.P2[G & 1]); }
template <int... Gs> DI void pv_last_(K1Ctx& c, iseq<Gs...>) { (pv_last_gap<Gs>(c), ...); }
template <int... Gs> DI void qfrag_(unsigned (&qa)[8], iseq<Gs...>) { (rd128<AQ + 4 * Gs, 256 * (Gs >> 3)>(qa[Gs & 7]), ...); }

template <int DT, int QT, int G> DI void qimg_write(char* qi, int w, int r, int h) {
  constexpr int A = 16 * (2 * DT + QT) + 4 * G;
  const int q = 32 * QT + r;
  const int ch = 8 * w + 4 * DT + G;
  u32x2 v = {cvtpk(ard<A>(), ard<A + 1>()), cvtpk(ard<A + 2>(), ard<A + 3>())};
  *(u32x2*)(qi + q * 512 + ((ch ^ (q & 15)) << 4) + 8 * h) = v;
}
template <int... Is> DI void qimg_all(char* qi, int w, int r, int h, iseq<Is...>) { (qimg_write<(Is >> 3), ((Is >> 2) & 1), (Is & 3)>(qi, w, r, h), ...); }

template <int KH, int I> DI void ep_send(char* dst) {
  constexpr int A = 16 * (4 * (1 - KH) + (I >> 2)) + 4 * (I & 3);
  f32x4 v = {ard<A>(), ard<A + 1>(), ard<A + 2>(), ard<A + 3>()};
  *(f32x4*)(dst + I * 1024) = v;
}
template <int KH, int... Is> DI void ep_send_all(char* dst, iseq<Is...>) { (ep_send<KH, Is>(dst), ...); }
template <int KH, int I> DI void ep_fin(const char* src, char* oi, int q, int h, float f1, float f2) {
  constexpr int t = I >> 2, g = I & 3, A = 16 * (4 * KH + t) + 4 * g;
  const f32x4 v = *(const f32x4*)(src + I * 1024);
  const float r0 = ard<A>() * f1 + v[0] * f2, r1 = ard<A + 1>() * f1 + v[1] * f2, r2 = ard<A + 2>() * f1 + v[2] * f2, r3 = ard<A + 3>() * f1 + v[3] * f2;
  const int ch = 4 * (4 * KH + t) + g;
  u32x2 pk = {cvtpk(r0, r1), cvtpk(r2, r3)};
  *(u32x2*)(oi + q * 512 + ((ch ^ (q & 15)) << 4) + 8 * h) = pk;
}
template <int KH, int... Is> DI void ep_fin_all(const char* src, char* oi, int q, int h, float f1, float f2, iseq<Is...>) { (ep_fin<KH, Is>(src, oi, q, h, f1, f2), ...); }

__global__ __launch_bounds__(256, 1) void mha_attn_kernel(const __hip_bfloat16* __restrict__ x, const __hip_bfloat16* __restrict__ kc,
                                                          const __hip_bfloat16* __restrict__ vc, const __hip_bfloat16* __restrict__ wq,
                                                          __hip_bfloat16* __restrict__ ows) {
  extern __shared__ __attribute__((aligned(16))) char lds[];
  const int tid = threadIdx.x, lane = tid & 63;
  const int w = __builtin_amdgcn_readfirstlane(tid >> 6);
  const int bid = blockIdx.x;
  const int xcd = bid & 7, idx = bid >> 3;
  const int head = xcd * 2 + (idx & 1), qh = (idx >> 1) & 1, b = idx >> 2;

  K1Ctx c;
  c.lds = lds; c.ldsa = (unsigned)(uintptr_t)lds; c.w = w; c.lane = lane;
  c.kg = (const char*)kc + (size_t)(b * NH + head) * SK * DH * 2;
  c.vg = (const char*)vc + (size_t)(b * NH + head) * SK * DH * 2;
  c.wg = (const char*)wq + head * DH * 2;

  {
    const char* xg = (const char*)x + (size_t)(b * SQ + qh * 64) * (DM * 2);
#pragma unroll
    for (int ck = 0; ck < 7; ++ck)
#pragma unroll
      for (int i = 0; i < 2; ++i) {
        const int row = (w * 2 + i) * 8 + (lane >> 3);
        const int ch = (lane & 7) ^ ((row >> 1) & 7);
        dma16(xg + ck * 128, row * (DM * 2) + ch * 16, c.ldsa + XOFF + ck * 8192 + (w * 2 + i) * 1024);
      }
  }
  issue_stage<0>(c); issue_stage<1>(c); issue_stage<2>(c);
  const int h = lane >> 5, r = lane & 31;
  {
    const int blk = (lane >> 4) & 1, qq = (lane & 15) >> 2, p = lane & 3, kh = w & 1;
    const int key = 32 * kh + r;
#pragma unroll
    for (int k = 0; k < 8; ++k) c.kaddr[k] = c.ldsa + key * 512 + (((2 * k + h) ^ (key & 15)) << 4);
#pragma unroll
    for (int t = 0; t < 4; ++t) c.vaddr[t] = c.ldsa + (32 * kh + 4 * h + qq) * 512 + 32 * blk + 8 * p + 64 * (t ^ qq);
#pragma unroll
    for (int dt = 0; dt < 2; ++dt) c.waddr[dt] = c.ldsa + (8 * h + qq) * 512 + 64 * ((2 * w + dt) ^ qq) + 32 * blk + 8 * p;
#pragma unroll
    for (int ks = 0; ks < 4; ++ks) c.xaddr[ks] = c.ldsa + XOFF + r * 128 + (((2 * ks + h) ^ ((r >> 1) & 7)) << 4);
  }
  c.pend = false; c.l = 0.f; c.alpha = 1.f; c.m = 0.f; c.ps0 = 0.f; c.ps1 = 0.f; c.pm = 0.f; c.mc = 0.f;

  wait_bar<16>(); GAP();
  wfrag0_(c, make_iseq<16>{}); GAP();
  qp_phase<1>(c); qp_phase<2>(c); qp_phase<3>(c); qp_phase<4>(c); qp_phase<5>(c); qp_phase<6>(c); qp_phase<7>(c);
  mfma_pad();
  qimg_all(lds + 3 * RSZ, w, r, h, make_iseq<16>{});
  wait_bar<32>(); GAP();
  {
    const int q = 32 * (w >> 1) + r;
    unsigned qa[8];
#pragma unroll
    for (int k = 0; k < 8; ++k) qa[k] = c.ldsa + 3 * RSZ + q * 512 + (((2 * k + h) ^ (q & 15)) << 4);
    qfrag_(qa, make_iseq<16>{});
  }
  wait_vm<32>(); GAP();

  a_phase<0>(c); b_phase<0>(c);
  a_phase<1>(c); b_phase<1>(c);
  a_phase<2>(c); b_phase<2>(c);
  a_phase<3>(c); b_phase<3>(c);
  a_phase<4>(c); b_phase<4>(c);
  a_phase<5>(c); b_phase<5>(c);
  a_phase<6>(c); b_phase<6>(c);
  a_phase<7>(c); b_phase<7>(c);
  wait_bar<0>(); GAP();
  pv_last_(c, make_iseq<16>{});
  mfma_pad();

  {
    const int kh = w & 1;
    const float m = c.m;
    const float l = swap_half_sum(c.l);
    char* ex = lds + 2 * RSZ;
    float* st = (float*)(lds + 4 * RSZ);
    st[w * 128 + lane] = m;
    st[w * 128 + 64 + lane] = l;
    char* dst = ex + w * 16384 + lane * 16;
    if (kh == 0) ep_send_all<0>(dst, make_iseq<16>{}); else ep_send_all<1>(dst, make_iseq<16>{});
    wait_bar<0>();
    const int pw = w ^ 1;
    const float m2 = st[pw * 128 + lane], l2 = st[pw * 128 + 64 + lane];
    const float mn = fmaxf(m, m2);
    const float a1 = __builtin_amdgcn_exp2f((m - mn) * SM_C), a2 = __builtin_amdgcn_exp2f((m2 - mn) * SM_C);
    const float inv = 1.0f / (l * a1 + l2 * a2);
    const float f1 = a1 * inv, f2 = a2 * inv;
    const char* src = ex + pw * 16384 + lane * 16;
    char* oi = lds;
    const int q = 32 * (w >> 1) + r;
    if (kh == 0) ep_fin_all<0>(src, oi, q, h, f1, f2, make_iseq<16>{}); else ep_fin_all<1>(src, oi, q, h, f1, f2, make_iseq<16>{});
    wait_bar<0>();
    char* og = (char*)ows + (size_t)(b * SQ + qh * 64) * (HD * 2) + head * (DH * 2);
#pragma unroll
    for (int i = 0; i < 8; ++i) {
      const int row = i * 8 + (tid >> 5), ch = tid & 31;
      const u32x4 v = *(const u32x4*)(oi + row * 512 + ((ch ^ (row & 15)) << 4));
      *(u32x4*)(og + (size_t)row * (HD * 2) + ch * 16) = v;
    }
  }
}
constexpr int K2_STG = 12288;
constexpr int K2_LDS = 4 * 3 * K2_STG;

DI void k2_issue(const char* ag, const char* bg, unsigned dst, int s, int lane) {
#pragma unroll
  for (int i = 0; i < 4; ++i) {
    const int row = i * 8 + (lane >> 3);
    const int ch = (lane & 7) ^ ((row >> 1) & 7);
    dma16(ag + s * 128, row * (HD * 2) + ch * 16, dst + i * 1024);
  }
#pragma unroll
  for (int i = 0; i < 8; ++i) {
    const int row = i * 8 + (lane >> 3);
    const int ch = (lane & 7) ^ (((row >> 1) & 1) << 2);
    dma16(bg + (size_t)(s * 64) * (DM * 2), row * (DM * 2) + ch * 16, dst + 4096 + i * 1024);
  }
}

__global__ __launch_bounds__(256, 1) void mha_oproj_kernel(const __hip_bfloat16* __restrict__ ows, const __hip_bfloat16* __restrict__ wo,
                                                           __hip_bfloat16* __restrict__ out) {
  extern __shared__ __attribute__((aligned(16))) char lds[];
  const int tid = threadIdx.x, lane = tid & 63;
  const int w = __builtin_amdgcn_readfirstlane(tid >> 6);
  const int bid = blockIdx.x;
  const int xcd = bid & 7, j = bid >> 3;
  const int mt = xcd * 4 + j / 7, nt = j % 7;
  const char* ag = (const char*)ows + (size_t)(mt * 32) * (HD * 2) + w * 2048;
  const char* bg = (const char*)wo + (size_t)(w * 1024) * (DM * 2) + nt * 128;
  char* ring = lds + w * 3 * K2_STG;
  const unsigned ringa = (unsigned)(uintptr_t)lds + w * 3 * K2_STG;
  const int h = lane >> 5, r = lane & 31, blk = (lane >> 4) & 1, qq = (lane & 15) >> 2, p = lane & 3;

  f32x16 acc[2];
#pragma unroll
  for (int i = 0; i < 2; ++i)
#pragma unroll
    for (int e = 0; e < 16; ++e) acc[i][e] = 0.f;
  k2_issue(ag, bg, ringa, 0, lane);
  k2_issue(ag, bg, ringa + K2_STG, 1, lane);
  int slot = 0;
  for (int s = 0; s < 16; ++s) {
    if (s < 15) wait_vm<12>(); else wait_vm<0>();
    if (s + 2 < 16) { const int ns = slot == 0 ? 2 : slot - 1; k2_issue(ag, bg, ringa + ns * K2_STG, s + 2, lane); }
    const char* ab = ring + slot * K2_STG;
    const char* bb = ab + 4096;
#pragma unroll
    for (int ks = 0; ks < 4; ++ks) {
      const bf16x8 a = *(const bf16x8*)(ab + r * 128 + (((2 * ks + h) ^ ((r >> 1) & 7)) << 4));
#pragma unroll
      for (int n2 = 0; n2 < 2; ++n2) {
        const char* ad = bb + (16 * ks + 8 * h + qq) * 128 + 64 * (n2 ^ ((qq >> 1) & 1)) + 32 * blk + 8 * p;
        const bf16x8 bf = cat8(tr_read(ad), tr_read(ad + 4 * 128));
        acc[n2] = MFMA(a, bf, acc[n2]);
      }
    }
    slot = slot == 2 ? 0 : slot + 1;
  }
  wait_bar<0>();
  float* red = (float*)lds;
#pragma unroll
  for (int n2 = 0; n2 < 2; ++n2)
#pragma unroll
    for (int e = 0; e < 16; ++e) {
      const int mrow = (e & 3) + 8 * (e >> 2) + 4 * h;
      red[w * 2048 + mrow * 64 + n2 * 32 + r] = acc[n2][e];
    }
  wait_bar<0>();
  {
    const int mrow = tid >> 3, n0 = (tid & 7) * 8;
    f32x4 s0 = {0.f, 0.f, 0.f, 0.f}, s1 = {0.f, 0.f, 0.f, 0.f};
#pragma unroll
    for (int ww = 0; ww < 4; ++ww) {
      s0 += *(const f32x4*)(red + ww * 2048 + mrow * 64 + n0);
      s1 += *(const f32x4*)(red + ww * 2048 + mrow * 64 + n0 + 4);
    }
    u32x4 pk = {cvtpk(s0[0], s0[1]), cvtpk(s0[2], s0[3]), cvtpk(s1[0], s1[1]), cvtpk(s1[2], s1[3])};
    *(u32x4*)((char*)out + (size_t)(mt * 32 + mrow) * (DM * 2) + nt * 128 + n0 * 2) = pk;
  }
}

extern "C" void kernel_launch(void* const* d_in, const int* in_sizes, int n_in, void* d_out, int out_size, void* d_ws, size_t ws_size,
                              hipStream_t stream) {
  const __hip_bfloat16* x = (const __hip_bfloat16*)d_in[0];
  const __hip_bfloat16* kc = (const __hip_bfloat16*)d_in[1];
  const __hip_bfloat16* vc = (const __hip_bfloat16*)d_in[2];
  const __hip_bfloat16* wq = (const __hip_bfloat16*)d_in[3];
  const __hip_bfloat16* wo = (const __hip_bfloat16*)d_in[4];
  __hip_bfloat16* out = (__hip_bfloat16*)d_out;
  __hip_bfloat16* ows = (__hip_bfloat16*)d_ws;
  (void)hipFuncSetAttribute((const void*)mha_attn_kernel, hipFuncAttributeMaxDynamicSharedMemorySize, K1_LDS);
  (void)hipFuncSetAttribute((const void*)mha_oproj_kernel, hipFuncAttributeMaxDynamicSharedMemorySize, K2_LDS);
  mha_attn_kernel<<<256, 256, K1_LDS, stream>>>(x, kc, vc, wq, ows);
  mha_oproj_kernel<<<224, 256, K2_LDS, stream>>>(ows, wo, out);
}
```

```cpp
#include <hip/hip_runtime.h>
#include <hip/hip_bf16.h>
#include <stdint.h>
#include <utility>

typedef __attribute__((ext_vector_type(8))) short bf16x8;
typedef __attribute__((ext_vector_type(4))) short s16x4;
typedef __attribute__((ext_vector_type(16))) float f32x16;
typedef __attribute__((ext_vector_type(4))) float f32x4;
typedef __attribute__((ext_vector_type(2))) float f32x2;
typedef __attribute__((ext_vector_type(4))) unsigned u32x4;
typedef __attribute__((ext_vector_type(2))) unsigned u32x2;
typedef __attribute__((ext_vector_type(2))) __bf16 bf16x2_t;
typedef __attribute__((address_space(3))) s16x4* lds_s16x4_ptr;

#define DI __device__ __forceinline__
#define MFMA(a, b, c) __builtin_amdgcn_mfma_f32_32x32x16_bf16((a), (b), (c), 0, 0, 0)
#define GAP() __builtin_amdgcn_sched_barrier(0)
#define ACLOB "a0","a1","a2","a3","a4","a5","a6","a7","a8","a9","a10","a11","a12","a13","a14","a15","a16","a17","a18","a19","a20","a21","a22","a23","a24","a25","a26","a27","a28","a29","a30","a31","a32","a33","a34","a35","a36","a37","a38","a39","a40","a41","a42","a43","a44","a45","a46","a47","a48","a49","a50","a51","a52","a53","a54","a55","a56","a57","a58","a59","a60","a61","a62","a63","a64","a65","a66","a67","a68","a69","a70","a71","a72","a73","a74","a75","a76","a77","a78","a79","a80","a81","a82","a83","a84","a85","a86","a87","a88","a89","a90","a91","a92","a93","a94","a95","a96","a97","a98","a99","a100","a101","a102","a103","a104","a105","a106","a107","a108","a109","a110","a111","a112","a113","a114","a115","a116","a117","a118","a119","a120","a121","a122","a123","a124","a125","a126","a127","a128","a129","a130","a131","a132","a133","a134","a135","a136","a137","a138","a139","a140","a141","a142","a143","a144","a145","a146","a147","a148","a149","a150","a151","a152","a153","a154","a155","a156","a157","a158","a159","a160","a161","a162","a163","a164","a165","a166","a167","a168","a169","a170","a171","a172","a173","a174","a175","a176","a177","a178","a179","a180","a181","a182","a183","a184","a185","a186","a187","a188","a189","a190","a191","a192","a193","a194","a195","a196","a197","a198","a199","a200","a201","a202","a203","a204","a205","a206","a207","a208","a209","a210","a211","a212","a213","a214","a215","a216","a217","a218","a219","a220","a221","a222","a223","a224","a225","a226","a227","a228","a229","a230","a231","a232","a233","a234","a235","a236","a237","a238","a239","a240","a241","a242","a243","a244","a245","a246","a247","a248","a249","a250","a251","a252","a253","a254","a255"
template <int... Is> using iseq = std::integer_sequence<int, Is...>;
template <int N> using make_iseq = std::make_integer_sequence<int, N>;

constexpr int NB = 8, SQ = 128, DM = 448, NH = 16, SK = 512, DH = 256;
constexpr int HD = NH * DH;

DI unsigned cvtpk(float lo, float hi) {
  f32x2 v = {lo, hi};
  bf16x2_t r = __builtin_convertvector(v, bf16x2_t);
  return __builtin_bit_cast(unsigned, r);
}
DI void dma16(const char* sbase, unsigned voff, unsigned ldsa) {
  asm volatile("s_mov_b32 m0, %2\n\ts_nop 0\n\tglobal_load_lds_dwordx4 %0, %1" ::"v"(voff), "s"(sbase), "s"(ldsa) : "memory");
}
DI s16x4 tr_read(const char* l) {
  return __builtin_amdgcn_ds_read_tr16_b64_v4i16((lds_s16x4_ptr)(l));
}
DI bf16x8 cat8(s16x4 a, s16x4 b) { return __builtin_shufflevector(a, b, 0, 1, 2, 3, 4, 5, 6, 7); }
template <int N> DI void wait_bar() {
  asm volatile("s_waitcnt vmcnt(%0) lgkmcnt(0)\n\ts_barrier" ::"n"(N) : "memory");
}
template <int N> DI void wait_vm() {
  asm volatile("s_waitcnt vmcnt(%0) lgkmcnt(0)" ::"n"(N) : "memory");
}
DI float swap_half_max(float v) {
  auto rr = __builtin_amdgcn_permlane32_swap(__float_as_uint(v), __float_as_uint(v), false, false);
  return fmaxf(__uint_as_float(rr[0]), __uint_as_float(rr[1]));
}
DI float swap_half_sum(float v) {
  auto rr = __builtin_amdgcn_permlane32_swap(__float_as_uint(v), __float_as_uint(v), false, false);
  return __uint_as_float(rr[0]) + __uint_as_float(rr[1]);
}
DI float max3f(float a, float b, float c) { float r; asm("v_max3_f32 %0, %1, %2, %3" : "=v"(r) : "v"(a), "v"(b), "v"(c)); return r; }
DI float max7(float p, float a, float b, float c, float d, float e, float f) {
  float r; asm("v_max3_f32 %0, %1, %2, %3\n\tv_max3_f32 %0, %0, %4, %5\n\tv_max3_f32 %0, %0, %6, %7" : "=&v"(r) : "v"(p), "v"(a), "v"(b), "v"(c), "v"(d), "v"(e), "v"(f)); return r;
}
DI void mfma_pad() { asm volatile("s_nop 7\n\ts_nop 7" ::: "memory"); }

template <int A, int OFF> DI void rd128(unsigned addr) {
  asm volatile("ds_read_b128 a[%c1:%c2], %0 offset:%c3" ::"v"(addr), "i"(A), "i"(A + 3), "i"(OFF) : "memory", ACLOB);
}
template <int A, int OFF> DI void rdtr(unsigned addr) {
  asm volatile("ds_read_b64_tr_b16 a[%c1:%c2], %0 offset:%c3" ::"v"(addr), "i"(A), "i"(A + 1), "i"(OFF) : "memory", ACLOB);
}
template <int A, int OFF0, int OFF1> DI void rdtr2(unsigned addr) {
  asm volatile("ds_read_b64_tr_b16 a[%c1:%c2], %0 offset:%c5\n\tds_read_b64_tr_b16 a[%c3:%c4], %0 offset:%c6" ::"v"(addr), "i"(A), "i"(A + 1), "i"(A + 2), "i"(A + 3), "i"(OFF0), "i"(OFF1) : "memory", ACLOB);
}
template <int A, int OFF> DI void wr128a(unsigned addr) {
  asm volatile("ds_write_b128 %0, a[%c1:%c2] offset:%c3" ::"v"(addr), "i"(A), "i"(A + 3), "i"(OFF) : "memory");
}
template <int OFF> DI void dmao(const char* sbase, unsigned voff) {
  asm volatile("global_load_lds_dwordx4 %0, %1 offset:%c2" ::"v"(voff), "s"(sbase), "i"(OFF) : "memory");
}
template <int OFF> DI void dmao_m0(const char* sbase, unsigned voff, unsigned ldsa) {
  asm volatile("s_mov_b32 m0, %2\n\ts_nop 0\n\tglobal_load_lds_dwordx4 %0, %1 offset:%c3" ::"v"(voff), "s"(sbase), "s"(ldsa), "i"(OFF) : "memory");
}
template <int D, int A, int B, bool ZERO> DI void mfma_aaa() {
  if constexpr (ZERO) asm volatile("v_mfma_f32_32x32x16_bf16 a[%c0:%c1], a[%c2:%c3], a[%c4:%c5], 0" ::"i"(D), "i"(D + 15), "i"(A), "i"(A + 3), "i"(B), "i"(B + 3) : ACLOB);
  else asm volatile("v_mfma_f32_32x32x16_bf16 a[%c0:%c1], a[%c2:%c3], a[%c4:%c5], a[%c0:%c1]" ::"i"(D), "i"(D + 15), "i"(A), "i"(A + 3), "i"(B), "i"(B + 3) : ACLOB);
}
template <int A, int B, bool ZERO> DI void mfma_vaa(f32x16& s) {
  if constexpr (ZERO) asm volatile("v_mfma_f32_32x32x16_bf16 %0, a[%c1:%c2], a[%c3:%c4], 0" : "=v"(s) : "i"(A), "i"(A + 3), "i"(B), "i"(B + 3) : ACLOB);
  else asm volatile("v_mfma_f32_32x32x16_bf16 %0, a[%c1:%c2], a[%c3:%c4], %0" : "+v"(s) : "i"(A), "i"(A + 3), "i"(B), "i"(B + 3) : ACLOB);
}
template <int D, int A, bool ZERO> DI void mfma_aav(const bf16x8& p) {
  if constexpr (ZERO) asm volatile("v_mfma_f32_32x32x16_bf16 a[%c1:%c2], a[%c3:%c4], %0, 0" ::"v"(p), "i"(D), "i"(D + 15), "i"(A), "i"(A + 3) : ACLOB);
  else asm volatile("v_mfma_f32_32x32x16_bf16 a[%c1:%c2], a[%c3:%c4], %0, a[%c1:%c2]" ::"v"(p), "i"(D), "i"(D + 15), "i"(A), "i"(A + 3) : ACLOB);
}
template <int A> DI float ard() { float r; asm volatile("v_accvgpr_read_b32 %0, a[%c1]" : "=v"(r) : "i"(A)); return r; }
template <int A> DI void ascale1(float f) {
  float t;
  asm volatile("v_accvgpr_read_b32 %0, a[%c2]\n\tv_mul_f32 %0, %1, %0\n\tv_accvgpr_write_b32 a[%c2], %0" : "=&v"(t) : "v"(f), "i"(A) : ACLOB);
}
template <int... Is> DI void ascale_all(float f, iseq<Is...>) { (ascale1<Is>(f), ...); }

constexpr int RSZ = 32768;
constexpr int K1_LDS = 5 * RSZ;
constexpr int XOFF = 3 * RSZ;
constexpr int AQ = 128, AF = 192;
constexpr float SM_C = 0.0625f * 1.4426950408889634f;
constexpr float SM_THR = 8.0f / 0.0625f;

struct K1Ctx {
  char* lds;
  unsigned ldsa;
  const char* kg;
  const char* vg;
  const char* wg;
  const char* xg;
  int w, lane;
  unsigned kaddr[8], vaddr[4], waddr[2], xaddr[4];
  unsigned kvo[8], vvo[4], wvo[8], xvo[2];
  unsigned dstw;
  f32x16 S[2];
  bf16x8 P[2], P2[2];
  float m, l, alpha, mc, ps0, ps1, pm;
  bool pend;
};

template <int TYPE, int I> DI void piece(const K1Ctx& c, const char* src, unsigned slot) {
  const unsigned vo = TYPE == 0 ? c.kvo[I] : TYPE == 1 ? c.vvo[(I & 1) + 2 * (I >> 2)] : c.wvo[I];
  if constexpr ((I & 3) == 0) dmao_m0<0>(src, vo, c.dstw + slot + (I >> 2) * 4096);
  else dmao<(I & 3) * 1024>(src, vo);
}
template <int TYPE, int... Is> DI void stage_(const K1Ctx& c, const char* src, unsigned slot, iseq<Is...>) { (piece<TYPE, Is>(c, src, slot), ...); }
template <int TYPE> DI void stage_all(const K1Ctx& c, const char* src, unsigned slot) { stage_<TYPE>(c, src, slot, make_iseq<8>{}); }
template <int S> DI void xpieces(const K1Ctx& c) {
  dmao_m0<0>(c.xg + S * 128, c.xvo[0], c.ldsa + XOFF + S * 8192 + c.w * 2048);
  dmao<1024>(c.xg + S * 128, c.xvo[1]);
}
template <int S> DI void wstage(const K1Ctx& c) { xpieces<S>(c); stage_all<2>(c, c.wg + (size_t)(S * 64) * (HD * 2), (S % 3) * RSZ); }

template <int J> DI void kfrag(const K1Ctx& c, unsigned rd) { rd128<AF + 4 * J, 256 * (J >> 3)>(c.kaddr[J & 7] + rd); }
template <int J> DI void vfrag(const K1Ctx& c, unsigned rd) {
  constexpr int t = J >> 1, ks = J & 1;
  rdtr2<AF + 4 * J, ks * 8192 + 256 * (t >> 2), ks * 8192 + 256 * (t >> 2) + 4096>(c.vaddr[t & 3] + rd);
}
__host__ __device__ constexpr int qset(int s) { return (s & 1) ? AF : AQ; }
template <int S, int J> DI void wfrag(const K1Ctx& c) {
  if constexpr (J < 8) {
    constexpr int ks = J >> 1, dt = J & 1;
    rdtr2<qset(S) + 4 * J, ks * 8192, ks * 8192 + 2048>(c.waddr[dt] + (S % 3) * RSZ);
  } else {
    constexpr int ks = (J - 8) >> 1, qt = (J - 8) & 1;
    rd128<qset(S) + 4 * J, qt * 4096>(c.xaddr[ks] + S * 8192);
  }
}
__host__ __device__ constexpr int worder(int g) { return (g & 3) < 2 ? 2 * (g >> 2) + (g & 1) : 8 + 2 * (g >> 2) + (g & 1); }

template <int P, int G> DI void qp_gap(K1Ctx& c) {
  constexpr int S = P - 1, ks = G >> 2, dt = (G >> 1) & 1, qt = G & 1;
  mfma_aaa<16 * (2 * dt + qt), qset(S) + 4 * (2 * ks + dt), qset(S) + 32 + 4 * (2 * ks + qt), (S == 0 && ks == 0)>();
  if constexpr (P < 7) wfrag<P, worder(G)>(c); else kfrag<G>(c, 1 * RSZ);
  if constexpr (P <= 4) {
    if constexpr (G == 0) xpieces<P + 2>(c);
    if constexpr (G >= 1 && G < 9) piece<2, G - 1>(c, c.wg + (size_t)((P + 2) * 64) * (HD * 2), ((P + 2) % 3) * RSZ);
  } else if constexpr (P == 5) { if constexpr (G < 8) piece<0, G>(c, c.kg, 1 * RSZ); }
  else if constexpr (P == 6) { if constexpr (G < 8) piece<0, G>(c, c.kg + RSZ, 2 * RSZ); }
  else { if constexpr (G < 8) piece<1, G>(c, c.vg, 0 * RSZ); else piece<0, G - 8>(c, c.kg + 2 * RSZ, 4 * RSZ); }
  GAP();
}
template <int P, int... Gs> DI void qp_phase_(K1Ctx& c, iseq<Gs...>) { (qp_gap<P, Gs>(c), ...); }
template <int P, int N> DI void qp_phase(K1Ctx& c) { wait_bar<N>(); GAP(); qp_phase_<P>(c, make_iseq<16>{}); }
template <int... Gs> DI void wfrag0_(const K1Ctx& c, iseq<Gs...>) { (wfrag<0, worder(Gs)>(c), ...); }

template <int X, int E> DI void sm_exp(K1Ctx& c) { c.S[X][E] = __builtin_amdgcn_exp2f(fmaf(c.S[X][E], SM_C, c.mc)); }
template <int X, int E> DI void sm_add(K1Ctx& c) { if constexpr (E & 1) c.ps1 += c.S[X][E]; else c.ps0 += c.S[X][E]; }
template <int X, int KS, bool LAST = false> DI void sm_pack(K1Ctx& c) {
  u32x4 w = {cvtpk(c.S[X][8 * KS + 0], c.S[X][8 * KS + 1]), cvtpk(c.S[X][8 * KS + 2], c.S[X][8 * KS + 3]),
             cvtpk(c.S[X][8 * KS + 4], c.S[X][8 * KS + 5]), cvtpk(c.S[X][8 * KS + 6], c.S[X][8 * KS + 7])};
  if constexpr (LAST) c.P2[KS] = __builtin_bit_cast(bf16x8, w); else c.P[KS] = __builtin_bit_cast(bf16x8, w);
}
template <bool FIRST> DI void sm_decide(K1Ctx& c) {
  const float pmax = swap_half_max(c.pm);
  if constexpr (FIRST) {
    c.m = pmax;
  } else {
    if (__builtin_expect(__any(pmax - c.m > SM_THR), 0)) {
      const float mn = fmaxf(c.m, pmax);
      c.alpha = __builtin_amdgcn_exp2f((c.m - mn) * SM_C);
      c.m = mn;
      c.l *= c.alpha;
      c.pend = true;
    }
  }
  c.mc = -c.m * SM_C;
  c.ps0 = 0.f; c.ps1 = 0.f;
}

template <int X, int KIND, int DMA, int G> DI void a_gap(K1Ctx& c, unsigned rd, const char* src, unsigned dst, bool dodma) {
  constexpr int Xp = X ^ 1;
  if constexpr (KIND != 2) mfma_vaa<AF + 4 * G, AQ + 4 * G, (G == 0)>(c.S[X]);
  if constexpr (KIND != 0) {
    vfrag<G>(c, rd);
    if constexpr (G < 8) { sm_exp<Xp, 8 + G>(c); if constexpr (G > 0) sm_add<Xp, 7 + G>(c); }
    if constexpr (G == 8) { sm_add<Xp, 15>(c); sm_pack<Xp, 0>(c); }
    if constexpr (G == 9) { sm_pack<Xp, 1>(c); c.l += c.ps0 + c.ps1; }
  }
  if constexpr (DMA == 1) { if constexpr (G >= 8) piece<1, G - 8>(c, src, dst); }
  if constexpr (DMA == 2) {
    if constexpr (G == 10) { if (dodma) { piece<1, 0>(c, src, dst); piece<1, 1>(c, src, dst); piece<1, 2>(c, src, dst); piece<1, 3>(c, src, dst); } }
    if constexpr (G == 13) { if (dodma) { piece<1, 4>(c, src, dst); piece<1, 5>(c, src, dst); piece<1, 6>(c, src, dst); piece<1, 7>(c, src, dst); } }
  }
  GAP();
}
template <int X, int KIND, int DMA, int... Gs> DI void a_phase(K1Ctx& c, unsigned rd, const char* src, unsigned dst, bool dodma, iseq<Gs...>) {
  (a_gap<X, KIND, DMA, Gs>(c, rd, src, dst, dodma), ...);
}
template <int X, int KIND, int DMA, int G> DI void b_gap(K1Ctx& c, unsigned rd, const char* src, unsigned dst, bool dodma) {
  if constexpr (KIND != 0) mfma_aav<16 * (G >> 1), AF + 4 * G, (KIND == 2 && (G & 1) == 0)>(c.P[G & 1]);
  if constexpr (KIND != 3) kfrag<G>(c, rd);
  if constexpr (G == 0) c.pm = max7(c.S[X][0], c.S[X][1], c.S[X][2], c.S[X][3], c.S[X][4], c.S[X][5], c.S[X][6]);
  if constexpr (G == 1) c.pm = max7(c.pm, c.S[X][7], c.S[X][8], c.S[X][9], c.S[X][10], c.S[X][11], c.S[X][12]);
  if constexpr (G == 2) c.pm = max7(c.pm, c.S[X][13], c.S[X][14], c.S[X][15], c.S[X][15], c.S[X][15], c.S[X][15]);
  if constexpr (G == 3) sm_decide<KIND == 0>(c);
  if constexpr (G >= 4 && G < 12) { sm_exp<X, G - 4>(c); if constexpr (G > 4) sm_add<X, G - 5>(c); }
  if constexpr (G == 12) sm_add<X, 7>(c);
  if constexpr (DMA == 1) { if constexpr (G >= 12) { piece<0, 2 * (G - 12)>(c, src, dst); piece<0, 2 * (G - 12) + 1>(c, src, dst); } }
  if constexpr (DMA == 2) {
    if constexpr (G == 12) { if (dodma) { piece<0, 0>(c, src, dst); piece<0, 1>(c, src, dst); piece<0, 2>(c, src, dst); piece<0, 3>(c, src, dst); } }
    if constexpr (G == 14) { if (dodma) { piece<0, 4>(c, src, dst); piece<0, 5>(c, src, dst); piece<0, 6>(c, src, dst); piece<0, 7>(c, src, dst); } }
  }
  GAP();
}
template <int X, int KIND, int DMA, int... Gs> DI void b_phase(K1Ctx& c, unsigned rd, const char* src, unsigned dst, bool dodma, iseq<Gs...>) {
  (b_gap<X, KIND, DMA, Gs>(c, rd, src, dst, dodma), ...);
  if constexpr (KIND != 0) {
    if (__builtin_expect(c.pend, 0)) { mfma_pad(); ascale_all(c.alpha, make_iseq<128>{}); asm volatile("s_nop 3"); c.pend = false; }
  }
}
template <int G> DI void pv_last_gap(K1Ctx& c) { mfma_aav<16 * (G >> 1), AF + 4 * G, false>(c.P[G & 1]); }
template <int... Gs> DI void pv_last_(K1Ctx& c, iseq<Gs...>) { (pv_last_gap<Gs>(c), ...); }
template <int... Gs> DI void qfrag_(unsigned (&qa)[8], iseq<Gs...>) { (rd128<AQ + 4 * Gs, 256 * (Gs >> 3)>(qa[Gs & 7]), ...); }

template <int DT, int QT, int G> DI void qimg_write(char* qi, unsigned qx) {
  constexpr int A = 16 * (2 * DT + QT) + 4 * G;
  u32x2 v = {cvtpk(ard<A>(), ard<A + 1>()), cvtpk(ard<A + 2>(), ard<A + 3>())};
  *(u32x2*)(qi + QT * 32 * 512 + (qx ^ ((4 * DT + G) << 4))) = v;
}
template <int... Is> DI void qimg_all(char* qi, unsigned qx, iseq<Is...>) { (qimg_write<(Is >> 3), ((Is >> 2) & 1), (Is & 3)>(qi, qx), ...); }

template <int KH, int I> DI void ep_send(unsigned dst) {
  wr128a<16 * (4 * (1 - KH) + (I >> 2)) + 4 * (I & 3), I * 1024>(dst);
}
template <int KH, int... Is> DI void ep_send_all(unsigned dst, iseq<Is...>) { (ep_send<KH, Is>(dst), ...); }
template <int KH, int I> DI void ep_fin(const char* src, char* oi, unsigned qx, float f1, float f2) {
  constexpr int t = I >> 2, g = I & 3, A = 16 * (4 * KH + t) + 4 * g;
  const f32x4 v = *(const f32x4*)(src + I * 1024);
  const float r0 = ard<A>() * f1 + v[0] * f2, r1 = ard<A + 1>() * f1 + v[1] * f2, r2 = ard<A + 2>() * f1 + v[2] * f2, r3 = ard<A + 3>() * f1 + v[3] * f2;
  u32x2 pk = {cvtpk(r0, r1), cvtpk(r2, r3)};
  *(u32x2*)(oi + (qx ^ ((4 * (4 * KH + t) + g) << 4))) = pk;
}
template <int KH, int... Is> DI void ep_fin_all(const char* src, char* oi, unsigned qx, float f1, float f2, iseq<Is...>) { (ep_fin<KH, Is>(src, oi, qx, f1, f2), ...); }

DI unsigned ring_off(int pos) { return ((0x40231u >> (4 * pos)) & 7u) << 15; }

__global__ __launch_bounds__(256, 1) void mha_attn_kernel(const __hip_bfloat16* __restrict__ x, const __hip_bfloat16* __restrict__ kc,
                                                          const __hip_bfloat16* __restrict__ vc, const __hip_bfloat16* __restrict__ wq,
                                                          __hip_bfloat16* __restrict__ ows) {
  extern __shared__ __attribute__((aligned(16))) char lds[];
  const int tid = threadIdx.x, lane = tid & 63;
  const int w = __builtin_amdgcn_readfirstlane(tid >> 6);
  const int bid = blockIdx.x;
  const int xcd = bid & 7, idx = bid >> 3;
  const int head = xcd * 2 + (idx & 1), qh = (idx >> 1) & 1, b = idx >> 2;
  const int h = lane >> 5, r = lane & 31;

  K1Ctx c;
  c.lds = lds; c.ldsa = (unsigned)(uintptr_t)lds; c.w = w; c.lane = lane;
  c.kg = (const char*)kc + (size_t)(b * NH + head) * SK * DH * 2 + w * 8192;
  c.vg = (const char*)vc + (size_t)(b * NH + head) * SK * DH * 2 + w * 8192;
  c.wg = (const char*)wq + head * DH * 2 + (size_t)(w * 16) * (HD * 2);
  c.xg = (const char*)x + (size_t)(b * SQ + qh * 64) * (DM * 2);
  c.dstw = c.ldsa + w * 8192;
#pragma unroll
  for (int i = 0; i < 8; ++i) {
    c.kvo[i] = h * 512 + ((r ^ (2 * i + h)) << 4) + (i >> 2) * 4096;
    c.wvo[i] = (2 * i + h) * (HD * 2) + ((r ^ (((2 * i + h) & 3) << 2)) << 4) - (i & 3) * 1024;
  }
#pragma unroll
  for (int i = 0; i < 4; ++i) c.vvo[i] = h * 512 + ((r ^ (((2 * (i & 1) + h) & 3) << 2)) << 4) + (i >> 1) * 4096;
#pragma unroll
  for (int i = 0; i < 2; ++i) {
    const int row = (w * 2 + i) * 8 + (lane >> 3);
    c.xvo[i] = row * (DM * 2) + (((lane & 7) ^ ((row >> 1) & 7)) << 4) - i * 1024 + 1024;
  }
  c.xg -= 1024;
  wstage<0>(c); wstage<1>(c); wstage<2>(c);
  {
    const int blk = (lane >> 4) & 1, qq = (lane & 15) >> 2, p = lane & 3, kh = w & 1;
    const int key = 32 * kh + r;
#pragma unroll
    for (int k = 0; k < 8; ++k) c.kaddr[k] = c.ldsa + key * 512 + (((2 * k + h) ^ (key & 15)) << 4);
#pragma unroll
    for (int t = 0; t < 4; ++t) c.vaddr[t] = c.ldsa + (32 * kh + 4 * h + qq) * 512 + 32 * blk + 8 * p + 64 * (t ^ qq);
#pragma unroll
    for (int dt = 0; dt < 2; ++dt) c.waddr[dt] = c.ldsa + (8 * h + qq) * 512 + 64 * ((2 * w + dt) ^ qq) + 32 * blk + 8 * p;
#pragma unroll
    for (int ks = 0; ks < 4; ++ks) c.xaddr[ks] = c.ldsa + XOFF + r * 128 + (((2 * ks + h) ^ ((r >> 1) & 7)) << 4);
  }
  c.pend = false; c.l = 0.f; c.alpha = 1.f; c.m = 0.f; c.ps0 = 0.f; c.ps1 = 0.f; c.pm = 0.f; c.mc = 0.f;

  wait_bar<20>(); GAP();
  wfrag0_(c, make_iseq<16>{}); GAP();
  qp_phase<1, 10>(c); qp_phase<2, 10>(c); qp_phase<3, 10>(c); qp_phase<4, 10>(c); qp_phase<5, 10>(c); qp_phase<6, 8>(c); qp_phase<7, 8>(c);
  mfma_pad();
  qimg_all(lds + 3 * RSZ, (unsigned)(r * 512 + 8 * h + (((r & 15) ^ (8 * w)) << 4)), make_iseq<16>{});
  wait_bar<32>(); GAP();
  {
    const int q = 32 * (w >> 1) + r;
    unsigned qa[8];
#pragma unroll
    for (int k = 0; k < 8; ++k) qa[k] = c.ldsa + 3 * RSZ + q * 512 + (((2 * k + h) ^ (q & 15)) << 4);
    qfrag_(qa, make_iseq<16>{});
  }
  wait_vm<32>(); GAP();

  typedef make_iseq<16> G16;
  a_phase<0, 0, 1>(c, 0, c.vg + RSZ, 1 * RSZ, true, G16{});
  mfma_pad(); wait_bar<24>(); GAP();
  b_phase<0, 0, 1>(c, 2 * RSZ, c.kg + 3 * RSZ, 3 * RSZ, true, G16{});
  wait_bar<24>(); GAP();
  a_phase<1, 1, 1>(c, 0 * RSZ, c.vg + 2 * RSZ, 2 * RSZ, true, G16{});
  mfma_pad(); wait_bar<24>(); GAP();
  b_phase<1, 2, 1>(c, 4 * RSZ, c.kg + 4 * RSZ, 0 * RSZ, true, G16{});
  int pos = 0;
  unsigned prev = 4 * RSZ;
  for (int t = 2; t < 6; t += 2) {
    unsigned r0 = ring_off(pos), r1 = ring_off(pos + 1 >= 5 ? pos - 4 : pos + 1), r2 = ring_off(pos + 2 >= 5 ? pos - 3 : pos + 2), r3 = ring_off(pos + 3 >= 5 ? pos - 2 : pos + 3);
    wait_bar<24>(); GAP();
    a_phase<0, 1, 1>(c, r0, c.vg + (size_t)(t + 1) * RSZ, prev, true, G16{});
    mfma_pad(); wait_bar<24>(); GAP();
    b_phase<0, 1, 1>(c, r1, c.kg + (size_t)(t + 3) * RSZ, r0, true, G16{});
    wait_bar<24>(); GAP();
    a_phase<1, 1, 1>(c, r2, c.vg + (size_t)(t + 2) * RSZ, r1, true, G16{});
    mfma_pad(); wait_bar<24>(); GAP();
    b_phase<1, 1, 2>(c, r3, c.kg + (size_t)(t + 4) * RSZ, r2, t + 4 < 8, G16{});
    prev = r3;
    pos = pos + 4 >= 5 ? pos - 1 : pos + 4;
  }
  wait_bar<16>(); GAP();
  a_phase<0, 1, 1>(c, 0 * RSZ, c.vg + 7 * RSZ, 2 * RSZ, true, G16{});
  mfma_pad(); wait_bar<16>(); GAP();
  b_phase<0, 1, 0>(c, 4 * RSZ, nullptr, 0, false, G16{});
  wait_bar<8>(); GAP();
  a_phase<1, 1, 0>(c, 1 * RSZ, nullptr, 0, false, G16{});
  mfma_pad(); wait_bar<8>(); GAP();
  b_phase<1, 3, 0>(c, 0, nullptr, 0, false, G16{});
  wait_bar<0>(); GAP();
  a_phase<0, 2, 0>(c, 2 * RSZ, nullptr, 0, false, G16{});
  wait_bar<0>(); GAP();
  pv_last_(c, G16{});
  mfma_pad();

  {
    const int kh = w & 1;
    const float m = c.m;
    const float l = swap_half_sum(c.l);
    char* ex = lds + 3 * RSZ;
    float* st = (float*)(lds + 1 * RSZ);
    st[w * 128 + lane] = m;
    st[w * 128 + 64 + lane] = l;
    const unsigned dst = c.ldsa + 3 * RSZ + w * 16384 + lane * 16;
    if (kh == 0) ep_send_all<0>(dst, G16{}); else ep_send_all<1>(dst, G16{});
    wait_bar<0>();
    const int pw = w ^ 1;
    const float m2 = st[pw * 128 + lane], l2 = st[pw * 128 + 64 + lane];
    const float mn = fmaxf(m, m2);
    const float a1 = __builtin_amdgcn_exp2f((m - mn) * SM_C), a2 = __builtin_amdgcn_exp2f((m2 - mn) * SM_C);
    const float inv = 1.0f / (l * a1 + l2 * a2);
    const float f1 = a1 * inv, f2 = a2 * inv;
    const char* src = ex + pw * 16384 + lane * 16;
    char* oi = lds;
    const int q = 32 * (w >> 1) + r;
    const unsigned qx = q * 512 + 8 * h + ((q & 15) << 4);
    if (kh == 0) ep_fin_all<0>(src, oi, qx, f1, f2, G16{}); else ep_fin_all<1>(src, oi, qx, f1, f2, G16{});
    wait_bar<0>();
    char* og = (char*)ows + (size_t)(b * SQ + qh * 64) * (HD * 2) + head * (DH * 2);
#pragma unroll
    for (int i = 0; i < 8; ++i) {
      const int row = i * 8 + (tid >> 5), ch = tid & 31;
      const u32x4 v = *(const u32x4*)(oi + row * 512 + ((ch ^ (row & 15)) << 4));
      *(u32x4*)(og + (size_t)row * (HD * 2) + ch * 16) = v;
    }
  }
}

constexpr int K2_STG = 12288;
constexpr int K2_LDS = 4 * 3 * K2_STG;

DI void k2_issue(const char* ag, const char* bg, unsigned dst, int s, int lane) {
#pragma unroll
  for (int i = 0; i < 4; ++i) {
    const int row = i * 8 + (lane >> 3);
    const int ch = (lane & 7) ^ ((row >> 1) & 7);
    dma16(ag + s * 128, row * (HD * 2) + ch * 16, dst + i * 1024);
  }
#pragma unroll
  for (int i = 0; i < 8; ++i) {
    const int row = i * 8 + (lane >> 3);
    const int ch = (lane & 7) ^ (((row >> 1) & 1) << 2);
    dma16(bg + (size_t)(s * 64) * (DM * 2), row * (DM * 2) + ch * 16, dst + 4096 + i * 1024);
  }
}

__global__ __launch_bounds__(256, 1) void mha_oproj_kernel(const __hip_bfloat16* __restrict__ ows, const __hip_bfloat16* __restrict__ wo,
                                                           __hip_bfloat16* __restrict__ out) {
  extern __shared__ __attribute__((aligned(16))) char lds[];
  const int tid = threadIdx.x, lane = tid & 63;
  const int w = __builtin_amdgcn_readfirstlane(tid >> 6);
  const int bid = blockIdx.x;
  const int xcd = bid & 7, j = bid >> 3;
  const int mt = xcd * 4 + j / 7, nt = j % 7;
  const char* ag = (const char*)ows + (size_t)(mt * 32) * (HD * 2) + w * 2048;
  const char* bg = (const char*)wo + (size_t)(w * 1024) * (DM * 2) + nt * 128;
  char* ring = lds + w * 3 * K2_STG;
  const unsigned ringa = (unsigned)(uintptr_t)lds + w * 3 * K2_STG;
  const int h = lane >> 5, r = lane & 31, blk = (lane >> 4) & 1, qq = (lane & 15) >> 2, p = lane & 3;

  f32x16 acc[2];
#pragma unroll
  for (int i = 0; i < 2; ++i)
#pragma unroll
    for (int e = 0; e < 16; ++e) acc[i][e] = 0.f;
  k2_issue(ag, bg, ringa, 0, lane);
  k2_issue(ag, bg, ringa + K2_STG, 1, lane);
  int slot = 0;
  for (int s = 0; s < 16; ++s) {
    if (s < 15) wait_vm<12>(); else wait_vm<0>();
    if (s + 2 < 16) { const int ns = slot == 0 ? 2 : slot - 1; k2_issue(ag, bg, ringa + ns * K2_STG, s + 2, lane); }
    const char* ab = ring + slot * K2_STG;
    const char* bb = ab + 4096;
#pragma unroll
    for (int ks = 0; ks < 4; ++ks) {
      const bf16x8 a = *(const bf16x8*)(ab + r * 128 + (((2 * ks + h) ^ ((r >> 1) & 7)) << 4));
#pragma unroll
      for (int n2 = 0; n2 < 2; ++n2) {
        const char* ad = bb + (16 * ks + 8 * h + qq) * 128 + 64 * (n2 ^ ((qq >> 1) & 1)) + 32 * blk + 8 * p;
        const bf16x8 bf = cat8(tr_read(ad), tr_read(ad + 4 * 128));
        acc[n2] = MFMA(a, bf, acc[n2]);
      }
    }
    slot = slot == 2 ? 0 : slot + 1;
  }
  wait_bar<0>();
  float* red = (float*)lds;
#pragma unroll
  for (int n2 = 0; n2 < 2; ++n2)
#pragma unroll
    for (int e = 0; e < 16; ++e) {
      const int mrow = (e & 3) + 8 * (e >> 2) + 4 * h;
      red[w * 2048 + mrow * 64 + n2 * 32 + r] = acc[n2][e];
    }
  wait_bar<0>();
  {
    const int mrow = tid >> 3, n0 = (tid & 7) * 8;
    f32x4 s0 = {0.f, 0.f, 0.f, 0.f}, s1 = {0.f, 0.f, 0.f, 0.f};
#pragma unroll
    for (int ww = 0; ww < 4; ++ww) {
      s0 += *(const f32x4*)(red + ww * 2048 + mrow * 64 + n0);
      s1 += *(const f32x4*)(red + ww * 2048 + mrow * 64 + n0 + 4);
    }
    u32x4 pk = {cvtpk(s0[0], s0[1]), cvtpk(s0[2], s0[3]), cvtpk(s1[0], s1[1]), cvtpk(s1[2], s1[3])};
    *(u32x4*)((char*)out + (size_t)(mt * 32 + mrow) * (DM * 2) + nt * 128 + n0 * 2) = pk;
  }
}

extern "C" void kernel_launch(void* const* d_in, const int* in_sizes, int n_in, void* d_out, int out_size, void* d_ws, size_t ws_size,
                              hipStream_t stream) {
  const __hip_bfloat16* x = (const __hip_bfloat16*)d_in[0];
  const __hip_bfloat16* kc = (const __hip_bfloat16*)d_in[1];
  const __hip_bfloat16* vc = (const __hip_bfloat16*)d_in[2];
  const __hip_bfloat16* wq = (const __hip_bfloat16*)d_in[3];
  const __hip_bfloat16* wo = (const __hip_bfloat16*)d_in[4];
  __hip_bfloat16* out = (__hip_bfloat16*)d_out;
  __hip_bfloat16* ows = (__hip_bfloat16*)d_ws;
  (void)hipFuncSetAttribute((const void*)mha_attn_kernel, hipFuncAttributeMaxDynamicSharedMemorySize, K1_LDS);
  (void)hipFuncSetAttribute((const void*)mha_oproj_kernel, hipFuncAttributeMaxDynamicSharedMemorySize, K2_LDS);
  mha_attn_kernel<<<256, 256, K1_LDS, stream>>>(x, kc, vc, wq, ows);
  mha_oproj_kernel<<<224, 256, K2_LDS, stream>>>(ows, wo, out);
}
```

```cpp
#include <hip/hip_runtime.h>
#include <hip/hip_bf16.h>
#include <stdint.h>
#include <utility>

typedef __attribute__((ext_vector_type(8))) short bf16x8;
typedef __attribute__((ext_vector_type(4))) short s16x4;
typedef __attribute__((ext_vector_type(16))) float f32x16;
typedef __attribute__((ext_vector_type(4))) float f32x4;
typedef __attribute__((ext_vector_type(2))) float f32x2;
typedef __attribute__((ext_vector_type(4))) unsigned u32x4;
typedef __attribute__((ext_vector_type(2))) unsigned u32x2;
typedef __attribute__((ext_vector_type(2))) __bf16 bf16x2_t;
typedef __attribute__((address_space(3))) s16x4* lds_s16x4_ptr;

#define DI __device__ __forceinline__
#define MFMA(a, b, c) __builtin_amdgcn_mfma_f32_32x32x16_bf16((a), (b), (c), 0, 0, 0)
#define GAP() __builtin_amdgcn_sched_barrier(0)
#define ACLOB "a0","a1","a2","a3","a4","a5","a6","a7","a8","a9","a10","a11","a12","a13","a14","a15","a16","a17","a18","a19","a20","a21","a22","a23","a24","a25","a26","a27","a28","a29","a30","a31","a32","a33","a34","a35","a36","a37","a38","a39","a40","a41","a42","a43","a44","a45","a46","a47","a48","a49","a50","a51","a52","a53","a54","a55","a56","a57","a58","a59","a60","a61","a62","a63","a64","a65","a66","a67","a68","a69","a70","a71","a72","a73","a74","a75","a76","a77","a78","a79","a80","a81","a82","a83","a84","a85","a86","a87","a88","a89","a90","a91","a92","a93","a94","a95","a96","a97","a98","a99","a100","a101","a102","a103","a104","a105","a106","a107","a108","a109","a110","a111","a112","a113","a114","a115","a116","a117","a118","a119","a120","a121","a122","a123","a124","a125","a126","a127","a128","a129","a130","a131","a132","a133","a134","a135","a136","a137","a138","a139","a140","a141","a142","a143","a144","a145","a146","a147","a148","a149","a150","a151","a152","a153","a154","a155","a156","a157","a158","a159","a160","a161","a162","a163","a164","a165","a166","a167","a168","a169","a170","a171","a172","a173","a174","a175","a176","a177","a178","a179","a180","a181","a182","a183","a184","a185","a186","a187","a188","a189","a190","a191","a192","a193","a194","a195","a196","a197","a198","a199","a200","a201","a202","a203","a204","a205","a206","a207","a208","a209","a210","a211","a212","a213","a214","a215","a216","a217","a218","a219","a220","a221","a222","a223","a224","a225","a226","a227","a228","a229","a230","a231","a232","a233","a234","a235","a236","a237","a238","a239","a240","a241","a242","a243","a244","a245","a246","a247","a248","a249","a250","a251","a252","a253","a254","a255"
template <int... Is> using iseq = std::integer_sequence<int, Is...>;
template <int N> using make_iseq = std::make_integer_sequence<int, N>;

constexpr int NB = 8, SQ = 128, DM = 448, NH = 16, SK = 512, DH = 256;
constexpr int HD = NH * DH;

DI unsigned cvtpk(float lo, float hi) {
  f32x2 v = {lo, hi};
  bf16x2_t r = __builtin_convertvector(v, bf16x2_t);
  return __builtin_bit_cast(unsigned, r);
}
DI void dma16(const char* sbase, unsigned voff, unsigned ldsa) {
  asm volatile("s_mov_b32 m0, %2\n\ts_nop 0\n\tglobal_load_lds_dwordx4 %0, %1" ::"v"(voff), "s"(sbase), "s"(ldsa) : "memory");
}
DI s16x4 tr_read(const char* l) {
  return __builtin_amdgcn_ds_read_tr16_b64_v4i16((lds_s16x4_ptr)(l));
}
DI bf16x8 cat8(s16x4 a, s16x4 b) { return __builtin_shufflevector(a, b, 0, 1, 2, 3, 4, 5, 6, 7); }
template <int N> DI void wait_bar() {
  asm volatile("s_waitcnt vmcnt(%0) lgkmcnt(0)\n\ts_barrier" ::"n"(N) : "memory");
}
template <int N> DI void wait_vm() {
  asm volatile("s_waitcnt vmcnt(%0) lgkmcnt(0)" ::"n"(N) : "memory");
}
DI float swap_half_max(float v) {
  auto rr = __builtin_amdgcn_permlane32_swap(__float_as_uint(v), __float_as_uint(v), false, false);
  return fmaxf(__uint_as_float(rr[0]), __uint_as_float(rr[1]));
}
DI float swap_half_sum(float v) {
  auto rr = __builtin_amdgcn_permlane32_swap(__float_as_uint(v), __float_as_uint(v), false, false);
  return __uint_as_float(rr[0]) + __uint_as_float(rr[1]);
}
DI float max3f(float a, float b, float c) { float r; asm("v_max3_f32 %0, %1, %2, %3" : "=v"(r) : "v"(a), "v"(b), "v"(c)); return r; }
DI float max7(float p, float a, float b, float c, float d, float e, float f) {
  float r; asm("v_max3_f32 %0, %1, %2, %3\n\tv_max3_f32 %0, %0, %4, %5\n\tv_max3_f32 %0, %0, %6, %7" : "=&v"(r) : "v"(p), "v"(a), "v"(b), "v"(c), "v"(d), "v"(e), "v"(f)); return r;
}
DI void mfma_pad() { asm volatile("s_nop 7\n\ts_nop 7" ::: "memory"); }

template <int A, int OFF> DI void rd128(unsigned addr) {
  asm volatile("ds_read_b128 a[%c1:%c2], %0 offset:%c3" ::"v"(addr), "i"(A), "i"(A + 3), "i"(OFF) : "memory", ACLOB);
}
template <int A, int OFF> DI void rdtr(unsigned addr) {
  asm volatile("ds_read_b64_tr_b16 a[%c1:%c2], %0 offset:%c3" ::"v"(addr), "i"(A), "i"(A + 1), "i"(OFF) : "memory", ACLOB);
}
template <int A, int OFF0, int OFF1> DI void rdtr2(unsigned addr) {
  asm volatile("ds_read_b64_tr_b16 a[%c1:%c2], %0 offset:%c5\n\tds_read_b64_tr_b16 a[%c3:%c4], %0 offset:%c6" ::"v"(addr), "i"(A), "i"(A + 1), "i"(A + 2), "i"(A + 3), "i"(OFF0), "i"(OFF1) : "memory", ACLOB);
}
template <int A, int OFF> DI void wr128a(unsigned addr) {
  asm volatile("ds_write_b128 %0, a[%c1:%c2] offset:%c3" ::"v"(addr), "i"(A), "i"(A + 3), "i"(OFF) : "memory");
}
template <int OFF> DI void dmao(const char* sbase, unsigned voff) {
  asm volatile("global_load_lds_dwordx4 %0, %1 offset:%c2" ::"v"(voff), "s"(sbase), "i"(OFF) : "memory");
}
template <int OFF> DI void dmao_m0(const char* sbase, unsigned voff, unsigned ldsa) {
  asm volatile("s_mov_b32 m0, %2\n\ts_nop 0\n\tglobal_load_lds_dwordx4 %0, %1 offset:%c3" ::"v"(voff), "s"(sbase), "s"(ldsa), "i"(OFF) : "memory");
}
template <int D, int A, int B, bool ZERO> DI void mfma_aaa() {
  if constexpr (ZERO) asm volatile("v_mfma_f32_32x32x16_bf16 a[%c0:%c1], a[%c2:%c3], a[%c4:%c5], 0" ::"i"(D), "i"(D + 15), "i"(A), "i"(A + 3), "i"(B), "i"(B + 3) : ACLOB);
  else asm volatile("v_mfma_f32_32x32x16_bf16 a[%c0:%c1], a[%c2:%c3], a[%c4:%c5], a[%c0:%c1]" ::"i"(D), "i"(D + 15), "i"(A), "i"(A + 3), "i"(B), "i"(B + 3) : ACLOB);
}
template <int A, int B, bool ZERO> DI void mfma_vaa(f32x16& s) {
  if constexpr (ZERO) asm volatile("v_mfma_f32_32x32x16_bf16 %0, a[%c1:%c2], a[%c3:%c4], 0" : "=v"(s) : "i"(A), "i"(A + 3), "i"(B), "i"(B + 3) : ACLOB);
  else asm volatile("v_mfma_f32_32x32x16_bf16 %0, a[%c1:%c2], a[%c3:%c4], %0" : "+v"(s) : "i"(A), "i"(A + 3), "i"(B), "i"(B + 3) : ACLOB);
}
template <int D, int A, bool ZERO> DI void mfma_aav(const bf16x8& p) {
  if constexpr (ZERO) asm volatile("v_mfma_f32_32x32x16_bf16 a[%c1:%c2], a[%c3:%c4], %0, 0" ::"v"(p), "i"(D), "i"(D + 15), "i"(A), "i"(A + 3) : ACLOB);
  else asm volatile("v_mfma_f32_32x32x16_bf16 a[%c1:%c2], a[%c3:%c4], %0, a[%c1:%c2]" ::"v"(p), "i"(D), "i"(D + 15), "i"(A), "i"(A + 3) : ACLOB);
}
template <int A> DI float ard() { float r; asm volatile("v_accvgpr_read_b32 %0, a[%c1]" : "=v"(r) : "i"(A)); return r; }
template <int A> DI void ascale1(float f) {
  float t;
  asm volatile("v_accvgpr_read_b32 %0, a[%c2]\n\tv_mul_f32 %0, %1, %0\n\tv_accvgpr_write_b32 a[%c2], %0" : "=&v"(t) : "v"(f), "i"(A) : ACLOB);
}
template <int... Is> DI void ascale_all(float f, iseq<Is...>) { (ascale1<Is>(f), ...); }

constexpr int RSZ = 32768;
constexpr int K1_LDS = 5 * RSZ;
constexpr int XOFF = 3 * RSZ;
constexpr int AQ = 128, AF = 192;
constexpr float SM_C = 0.0625f * 1.4426950408889634f;
constexpr float SM_THR = 8.0f / 0.0625f;

struct K1Ctx {
  char* lds;
  unsigned ldsa;
  const char* kg;
  const char* vg;
  const char* wg;
  const char* xg;
  int w, lane;
  unsigned kaddr[8], vaddr[4], waddr[2], xaddr[4];
  unsigned kvo[8], vvo[4], wvo[8], xvo[2];
  unsigned dstw;
  const char* ug;
  unsigned uvo[7], uaddr[2], dstu;
  f32x16 S[2];
  bf16x8 P[2], P2[2];
  float m, l, alpha, mc, ps0, ps1, pm;
  bool pend;
};

template <int TYPE, int I> DI void piece(const K1Ctx& c, const char* src, unsigned slot) {
  const unsigned vo = TYPE == 0 ? c.kvo[I] : TYPE == 1 ? c.vvo[(I & 1) + 2 * (I >> 2)] : c.wvo[I];
  if constexpr ((I & 3) == 0) dmao_m0<0>(src, vo, c.dstw + slot + (I >> 2) * 4096);
  else dmao<(I & 3) * 1024>(src, vo);
}
template <int TYPE, int... Is> DI void stage_(const K1Ctx& c, const char* src, unsigned slot, iseq<Is...>) { (piece<TYPE, Is>(c, src, slot), ...); }
template <int TYPE> DI void stage_all(const K1Ctx& c, const char* src, unsigned slot) { stage_<TYPE>(c, src, slot, make_iseq<8>{}); }
template <int S> DI void xpieces(const K1Ctx& c) {
  dmao_m0<0>(c.xg + S * 128, c.xvo[0], c.ldsa + XOFF + S * 8192 + c.w * 2048);
  dmao<1024>(c.xg + S * 128, c.xvo[1]);
}
template <int S> DI void wstage(const K1Ctx& c) { xpieces<S>(c); stage_all<2>(c, c.wg + (size_t)(S * 64) * (HD * 2), (S % 3) * RSZ); }

template <int J> DI void kfrag(const K1Ctx& c, unsigned rd) { rd128<AF + 4 * J, 256 * (J >> 3)>(c.kaddr[J & 7] + rd); }
template <int J> DI void vfrag(const K1Ctx& c, unsigned rd) {
  constexpr int t = J >> 1, ks = J & 1;
  rdtr2<AF + 4 * J, ks * 8192 + 256 * (t >> 2), ks * 8192 + 256 * (t >> 2) + 4096>(c.vaddr[t & 3] + rd);
}
__host__ __device__ constexpr int qset(int s) { return (s & 1) ? AF : AQ; }
template <int S, int J> DI void wfrag(const K1Ctx& c) {
  if constexpr (J < 8) {
    constexpr int ks = J >> 1, dt = J & 1;
    rdtr2<qset(S) + 4 * J, ks * 8192, ks * 8192 + 2048>(c.waddr[dt] + (S % 3) * RSZ);
  } else {
    constexpr int ks = (J - 8) >> 1, qt = (J - 8) & 1;
    rd128<qset(S) + 4 * J, qt * 4096>(c.xaddr[ks] + S * 8192);
  }
}
__host__ __device__ constexpr int worder(int g) { return (g & 3) < 2 ? 2 * (g >> 2) + (g & 1) : 8 + 2 * (g >> 2) + (g & 1); }

template <int P, int G> DI void qp_gap(K1Ctx& c) {
  constexpr int S = P - 1, ks = G >> 2, dt = (G >> 1) & 1, qt = G & 1;
  mfma_aaa<16 * (2 * dt + qt), qset(S) + 4 * (2 * ks + dt), qset(S) + 32 + 4 * (2 * ks + qt), (S == 0 && ks == 0)>();
  if constexpr (P < 7) wfrag<P, worder(G)>(c); else kfrag<G>(c, 1 * RSZ);
  if constexpr (P <= 4) {
    if constexpr (G == 0) xpieces<P + 2>(c);
    if constexpr (G >= 1 && G < 9) piece<2, G - 1>(c, c.wg + (size_t)((P + 2) * 64) * (HD * 2), ((P + 2) % 3) * RSZ);
  } else if constexpr (P == 5) { if constexpr (G < 8) piece<0, G>(c, c.kg, 1 * RSZ); }
  else if constexpr (P == 6) { if constexpr (G < 8) piece<0, G>(c, c.kg + RSZ, 2 * RSZ); }
  else { if constexpr (G < 8) piece<1, G>(c, c.vg, 0 * RSZ); else piece<0, G - 8>(c, c.kg + 2 * RSZ, 4 * RSZ); }
  GAP();
}
template <int P, int... Gs> DI void qp_phase_(K1Ctx& c, iseq<Gs...>) { (qp_gap<P, Gs>(c), ...); }
template <int P, int N> DI void qp_phase(K1Ctx& c) { wait_bar<N>(); GAP(); qp_phase_<P>(c, make_iseq<16>{}); }
template <int... Gs> DI void wfrag0_(const K1Ctx& c, iseq<Gs...>) { (wfrag<0, worder(Gs)>(c), ...); }

template <int X, int E> DI void sm_exp(K1Ctx& c) { c.S[X][E] = __builtin_amdgcn_exp2f(fmaf(c.S[X][E], SM_C, c.mc)); }
template <int X, int E> DI void sm_add(K1Ctx& c) { if constexpr (E & 1) c.ps1 += c.S[X][E]; else c.ps0 += c.S[X][E]; }
template <int X, int KS, bool LAST = false> DI void sm_pack(K1Ctx& c) {
  u32x4 w = {cvtpk(c.S[X][8 * KS + 0], c.S[X][8 * KS + 1]), cvtpk(c.S[X][8 * KS + 2], c.S[X][8 * KS + 3]),
             cvtpk(c.S[X][8 * KS + 4], c.S[X][8 * KS + 5]), cvtpk(c.S[X][8 * KS + 6], c.S[X][8 * KS + 7])};
  if constexpr (LAST) c.P2[KS] = __builtin_bit_cast(bf16x8, w); else c.P[KS] = __builtin_bit_cast(bf16x8, w);
}
template <bool FIRST> DI void sm_decide(K1Ctx& c) {
  const float pmax = swap_half_max(c.pm);
  if constexpr (FIRST) {
    c.m = pmax;
  } else {
    if (__builtin_expect(__any(pmax - c.m > SM_THR), 0)) {
      const float mn = fmaxf(c.m, pmax);
      c.alpha = __builtin_amdgcn_exp2f((c.m - mn) * SM_C);
      c.m = mn;
      c.l *= c.alpha;
      c.pend = true;
    }
  }
  c.mc = -c.m * SM_C;
  c.ps0 = 0.f; c.ps1 = 0.f;
}

template <int I> DI void upiece(const K1Ctx& c, int s, unsigned slot) {
  const char* src = c.ug + (size_t)s * 28672;
  if constexpr ((I & 3) == 0) dmao_m0<0>(src, c.uvo[I], c.dstu + slot + (I >> 2) * 4096);
  else dmao<(I & 3) * 1024>(src, c.uvo[I]);
}
template <int G> DI void ufrag(const K1Ctx& c, unsigned slot) {
  constexpr int ksl = G / 7, j = G % 7;
  rdtr2<AF + 4 * G, ksl * 14336 + 64 * j, ksl * 14336 + 64 * j + 7168>(c.uaddr[j & 1] + slot);
}
template <int... Gs> DI void ufrag_(const K1Ctx& c, unsigned slot, iseq<Gs...>) { (ufrag<Gs>(c, slot), ...); }
__host__ __device__ constexpr int uslot(int s) { return s == 0 ? 3 : s == 1 ? 0 : s == 2 ? 4 : s == 3 ? 1 : s == 4 ? 2 : s == 5 ? 3 : s == 6 ? 0 : 4; }
template <int S, int G> DI void c_gap(K1Ctx& c) {
  constexpr int ksl = G / 7, j = G % 7;
  mfma_aaa<16 * j, AF + 4 * G, AQ + 4 * (2 * S + ksl), (S == 0 && ksl == 0)>();
  if constexpr (S < 7) ufrag<G>(c, uslot(S + 1) * RSZ);
  if constexpr (S == 0) { if constexpr (G < 7) upiece<G>(c, 3, uslot(3) * RSZ); else upiece<G - 7>(c, 4, uslot(4) * RSZ); }
  if constexpr (S == 1) { if constexpr (G < 7) upiece<G>(c, 5, uslot(5) * RSZ); }
  if constexpr (S == 2) { if constexpr (G < 7) upiece<G>(c, 6, uslot(6) * RSZ); else upiece<G - 7>(c, 7, uslot(7) * RSZ); }
  GAP();
}
template <int S, int... Gs> DI void c_phase_(K1Ctx& c, iseq<Gs...>) { (c_gap<S, Gs>(c), ...); }
template <int S, int N> DI void c_phase(K1Ctx& c) { wait_bar<N>(); GAP(); c_phase_<S>(c, make_iseq<14>{}); }
template <int... Is> DI void ustage_(const K1Ctx& c, int s, unsigned slot, iseq<Is...>) { (upiece<Is>(c, s, slot), ...); }

template <int X, int KIND, int DMA, int G> DI void a_gap(K1Ctx& c, unsigned rd, const char* src, unsigned dst, bool dodma) {
  constexpr int Xp = X ^ 1;
  if constexpr (KIND != 2) mfma_vaa<AF + 4 * G, AQ + 4 * G, (G == 0)>(c.S[X]);
  if constexpr (KIND != 0) {
    vfrag<G>(c, rd);
    if constexpr (G < 8) { sm_exp<Xp, 8 + G>(c); if constexpr (G > 0) sm_add<Xp, 7 + G>(c); }
    if constexpr (G == 8) { sm_add<Xp, 15>(c); sm_pack<Xp, 0>(c); }
    if constexpr (G == 9) { sm_pack<Xp, 1>(c); c.l += c.ps0 + c.ps1; }
  }
  if constexpr (DMA == 1) { if constexpr (G >= 8) piece<1, G - 8>(c, src, dst); }
  if constexpr (DMA == 3) { if constexpr (G >= 8 && G < 15) upiece<G - 8>(c, 2, dst); }
  if constexpr (DMA == 2) {
    if constexpr (G == 10) { if (dodma) { piece<1, 0>(c, src, dst); piece<1, 1>(c, src, dst); piece<1, 2>(c, src, dst); piece<1, 3>(c, src, dst); } }
    if constexpr (G == 13) { if (dodma) { piece<1, 4>(c, src, dst); piece<1, 5>(c, src, dst); piece<1, 6>(c, src, dst); piece<1, 7>(c, src, dst); } }
  }
  GAP();
}
template <int X, int KIND, int DMA, int... Gs> DI void a_phase(K1Ctx& c, unsigned rd, const char* src, unsigned dst, bool dodma, iseq<Gs...>) {
  (a_gap<X, KIND, DMA, Gs>(c, rd, src, dst, dodma), ...);
}
template <int X, int KIND, int DMA, int G> DI void b_gap(K1Ctx& c, unsigned rd, const char* src, unsigned dst, bool dodma) {
  if constexpr (KIND != 0) mfma_aav<16 * (G >> 1), AF + 4 * G, (KIND == 2 && (G & 1) == 0)>(c.P[G & 1]);
  if constexpr (KIND != 3) kfrag<G>(c, rd);
  if constexpr (G == 0) c.pm = max7(c.S[X][0], c.S[X][1], c.S[X][2], c.S[X][3], c.S[X][4], c.S[X][5], c.S[X][6]);
  if constexpr (G == 1) c.pm = max7(c.pm, c.S[X][7], c.S[X][8], c.S[X][9], c.S[X][10], c.S[X][11], c.S[X][12]);
  if constexpr (G == 2) c.pm = max7(c.pm, c.S[X][13], c.S[X][14], c.S[X][15], c.S[X][15], c.S[X][15], c.S[X][15]);
  if constexpr (G == 3) sm_decide<KIND == 0>(c);
  if constexpr (G >= 4 && G < 12) { sm_exp<X, G - 4>(c); if constexpr (G > 4) sm_add<X, G - 5>(c); }
  if constexpr (G == 12) sm_add<X, 7>(c);
  if constexpr (DMA == 1) { if constexpr (G >= 12) { piece<0, 2 * (G - 12)>(c, src, dst); piece<0, 2 * (G - 12) + 1>(c, src, dst); } }
  if constexpr (DMA == 3) { if constexpr (G >= 9) upiece<G - 9>(c, 1, dst); }
  if constexpr (DMA == 2) {
    if constexpr (G == 12) { if (dodma) { piece<0, 0>(c, src, dst); piece<0, 1>(c, src, dst); piece<0, 2>(c, src, dst); piece<0, 3>(c, src, dst); } else { upiece<0>(c, 0, dst); upiece<1>(c, 0, dst); upiece<2>(c, 0, dst); upiece<3>(c, 0, dst); } }
    if constexpr (G == 14) { if (dodma) { piece<0, 4>(c, src, dst); piece<0, 5>(c, src, dst); piece<0, 6>(c, src, dst); piece<0, 7>(c, src, dst); } else { upiece<4>(c, 0, dst); upiece<5>(c, 0, dst); upiece<6>(c, 0, dst); } }
  }
  GAP();
}
template <int X, int KIND, int DMA, int... Gs> DI void b_phase(K1Ctx& c, unsigned rd, const char* src, unsigned dst, bool dodma, iseq<Gs...>) {
  (b_gap<X, KIND, DMA, Gs>(c, rd, src, dst, dodma), ...);
  if constexpr (KIND != 0) {
    if (__builtin_expect(c.pend, 0)) { mfma_pad(); ascale_all(c.alpha, make_iseq<128>{}); asm volatile("s_nop 3"); c.pend = false; }
  }
}
template <int G> DI void pv_last_gap(K1Ctx& c) { mfma_aav<16 * (G >> 1), AF + 4 * G, false>(c.P[G & 1]); }
template <int... Gs> DI void pv_last_(K1Ctx& c, iseq<Gs...>) { (pv_last_gap<Gs>(c), ...); }
template <int... Gs> DI void qfrag_(unsigned (&qa)[8], iseq<Gs...>) { (rd128<AQ + 4 * Gs, 256 * (Gs >> 3)>(qa[Gs & 7]), ...); }

template <int DT, int QT, int G> DI void qimg_write(char* qi, unsigned qx) {
  constexpr int A = 16 * (2 * DT + QT) + 4 * G;
  u32x2 v = {cvtpk(ard<A>(), ard<A + 1>()), cvtpk(ard<A + 2>(), ard<A + 3>())};
  *(u32x2*)(qi + QT * 32 * 512 + (qx ^ ((4 * DT + G) << 4))) = v;
}
template <int... Is> DI void qimg_all(char* qi, unsigned qx, iseq<Is...>) { (qimg_write<(Is >> 3), ((Is >> 2) & 1), (Is & 3)>(qi, qx), ...); }

template <int KH, int I> DI void ep_send(unsigned dst) {
  wr128a<16 * (4 * (1 - KH) + (I >> 2)) + 4 * (I & 3), I * 1024>(dst);
}
template <int KH, int... Is> DI void ep_send_all(unsigned dst, iseq<Is...>) { (ep_send<KH, Is>(dst), ...); }
template <int KH, int F> DI void ep_fin(char* buf, float f1, float f2) {
  constexpr int t = F >> 1, i = F & 1, A = 16 * (4 * KH + t) + 8 * i;
  const f32x4 v = *(const f32x4*)(buf + (4 * t + 2 * i) * 1024);
  const f32x4 u = *(const f32x4*)(buf + (4 * t + 2 * i + 1) * 1024);
  const unsigned p0 = cvtpk(ard<A>() * f1 + v[0] * f2, ard<A + 1>() * f1 + v[1] * f2), p1 = cvtpk(ard<A + 2>() * f1 + v[2] * f2, ard<A + 3>() * f1 + v[3] * f2);
  const unsigned p2 = cvtpk(ard<A + 4>() * f1 + u[0] * f2, ard<A + 5>() * f1 + u[1] * f2), p3 = cvtpk(ard<A + 6>() * f1 + u[2] * f2, ard<A + 7>() * f1 + u[3] * f2);
  u32x4 pk = {p0, p1, p2, p3};
  *(u32x4*)(buf + F * 1024) = pk;
}
template <int KH, int... Is> DI void ep_fin_all(char* buf, float f1, float f2, iseq<Is...>) { (ep_fin<KH, Is>(buf, f1, f2), ...); }
template <int... Gs> DI void ofrag_(unsigned a0, unsigned a1, iseq<Gs...>) { (rd128<AQ + 4 * Gs, 1024 * (Gs & 7)>(Gs < 8 ? a0 : a1), ...); }
template <int I> DI void out_store(char* dst) {
  constexpr int A = 8 * I;
  u32x4 pk = {cvtpk(ard<A>(), ard<A + 1>()), cvtpk(ard<A + 2>(), ard<A + 3>()), cvtpk(ard<A + 4>(), ard<A + 5>()), cvtpk(ard<A + 6>(), ard<A + 7>())};
  *(u32x4*)(dst + I * 1024) = pk;
}
template <int... Is> DI void out_store_(char* dst, iseq<Is...>) { (out_store<Is>(dst), ...); }

DI unsigned ring_off(int pos) { return ((0x40231u >> (4 * pos)) & 7u) << 15; }

__global__ __launch_bounds__(256, 1) void mha_attn_kernel(const __hip_bfloat16* __restrict__ x, const __hip_bfloat16* __restrict__ kc,
                                                          const __hip_bfloat16* __restrict__ vc, const __hip_bfloat16* __restrict__ wq,
                                                          const __hip_bfloat16* __restrict__ wo, __hip_bfloat16* __restrict__ pws) {
  extern __shared__ __attribute__((aligned(16))) char lds[];
  const int tid = threadIdx.x, lane = tid & 63;
  const int w = __builtin_amdgcn_readfirstlane(tid >> 6);
  const int bid = blockIdx.x;
  const int xcd = bid & 7, idx = bid >> 3;
  const int head = xcd * 2 + (idx & 1), qh = (idx >> 1) & 1, b = idx >> 2;
  const int h = lane >> 5, r = lane & 31;

  K1Ctx c;
  c.lds = lds; c.ldsa = (unsigned)(uintptr_t)lds; c.w = w; c.lane = lane;
  c.kg = (const char*)kc + (size_t)(b * NH + head) * SK * DH * 2 + w * 8192;
  c.vg = (const char*)vc + (size_t)(b * NH + head) * SK * DH * 2 + w * 8192;
  c.wg = (const char*)wq + head * DH * 2 + (size_t)(w * 16) * (HD * 2);
  c.xg = (const char*)x + (size_t)(b * SQ + qh * 64) * (DM * 2);
  c.dstw = c.ldsa + w * 8192;
#pragma unroll
  for (int i = 0; i < 8; ++i) {
    c.kvo[i] = h * 512 + ((r ^ (2 * i + h)) << 4) + (i >> 2) * 4096;
    c.wvo[i] = (2 * i + h) * (HD * 2) + ((r ^ (((2 * i + h) & 3) << 2)) << 4) - (i & 3) * 1024;
  }
#pragma unroll
  for (int i = 0; i < 4; ++i) c.vvo[i] = h * 512 + ((r ^ (((2 * (i & 1) + h) & 3) << 2)) << 4) + (i >> 1) * 4096;
#pragma unroll
  for (int i = 0; i < 2; ++i) {
    const int row = (w * 2 + i) * 8 + (lane >> 3);
    c.xvo[i] = row * (DM * 2) + (((lane & 7) ^ ((row >> 1) & 7)) << 4) - i * 1024 + 1024;
  }
  c.xg -= 1024;
  c.ug = (const char*)wo + (size_t)head * DH * DM * 2 - 1024;
  c.dstu = c.ldsa + w * 7168;
#pragma unroll
  for (int i = 0; i < 7; ++i) {
    const int p = (7 * w + i) * 64 + lane, row = p / 56, ch = p - row * 56;
    c.uvo[i] = row * 896 + ((ch ^ (((row >> 1) & 1) << 2)) << 4) - (i & 3) * 1024 + 1024;
  }
  wstage<0>(c); wstage<1>(c); wstage<2>(c);
  {
    const int blk = (lane >> 4) & 1, qq = (lane & 15) >> 2, p = lane & 3, kh = w & 1;
    const int key = 32 * kh + r;
#pragma unroll
    for (int k = 0; k < 8; ++k) c.kaddr[k] = c.ldsa + key * 512 + (((2 * k + h) ^ (key & 15)) << 4);
#pragma unroll
    for (int t = 0; t < 4; ++t) c.vaddr[t] = c.ldsa + (32 * kh + 4 * h + qq) * 512 + 32 * blk + 8 * p + 64 * (t ^ qq);
#pragma unroll
    for (int dt = 0; dt < 2; ++dt) c.waddr[dt] = c.ldsa + (8 * h + qq) * 512 + 64 * ((2 * w + dt) ^ qq) + 32 * blk + 8 * p;
#pragma unroll
    for (int ks = 0; ks < 4; ++ks) c.xaddr[ks] = c.ldsa + XOFF + r * 128 + (((2 * ks + h) ^ ((r >> 1) & 7)) << 4);
  }
  c.pend = false; c.l = 0.f; c.alpha = 1.f; c.m = 0.f; c.ps0 = 0.f; c.ps1 = 0.f; c.pm = 0.f; c.mc = 0.f;

  wait_bar<20>(); GAP();
  wfrag0_(c, make_iseq<16>{}); GAP();
  qp_phase<1, 10>(c); qp_phase<2, 10>(c); qp_phase<3, 10>(c); qp_phase<4, 10>(c); qp_phase<5, 10>(c); qp_phase<6, 8>(c); qp_phase<7, 8>(c);
  mfma_pad();
  qimg_all(lds + 3 * RSZ, (unsigned)(r * 512 + 8 * h + (((r & 15) ^ (8 * w)) << 4)), make_iseq<16>{});
  wait_bar<32>(); GAP();
  {
    const int q = 32 * (w >> 1) + r;
    unsigned qa[8];
#pragma unroll
    for (int k = 0; k < 8; ++k) qa[k] = c.ldsa + 3 * RSZ + q * 512 + (((2 * k + h) ^ (q & 15)) << 4);
    qfrag_(qa, make_iseq<16>{});
  }
  wait_vm<32>(); GAP();

  typedef make_iseq<16> G16;
  a_phase<0, 0, 1>(c, 0, c.vg + RSZ, 1 * RSZ, true, G16{});
  mfma_pad(); wait_bar<24>(); GAP();
  b_phase<0, 0, 1>(c, 2 * RSZ, c.kg + 3 * RSZ, 3 * RSZ, true, G16{});
  wait_bar<24>(); GAP();
  a_phase<1, 1, 1>(c, 0 * RSZ, c.vg + 2 * RSZ, 2 * RSZ, true, G16{});
  mfma_pad(); wait_bar<24>(); GAP();
  b_phase<1, 2, 1>(c, 4 * RSZ, c.kg + 4 * RSZ, 0 * RSZ, true, G16{});
  int pos = 0;
  unsigned prev = 4 * RSZ;
  for (int t = 2; t < 6; t += 2) {
    unsigned r0 = ring_off(pos), r1 = ring_off(pos + 1 >= 5 ? pos - 4 : pos + 1), r2 = ring_off(pos + 2 >= 5 ? pos - 3 : pos + 2), r3 = ring_off(pos + 3 >= 5 ? pos - 2 : pos + 3);
    wait_bar<24>(); GAP();
    a_phase<0, 1, 1>(c, r0, c.vg + (size_t)(t + 1) * RSZ, prev, true, G16{});
    mfma_pad(); wait_bar<24>(); GAP();
    b_phase<0, 1, 1>(c, r1, c.kg + (size_t)(t + 3) * RSZ, r0, true, G16{});
    wait_bar<24>(); GAP();
    a_phase<1, 1, 1>(c, r2, c.vg + (size_t)(t + 2) * RSZ, r1, true, G16{});
    mfma_pad(); wait_bar<24>(); GAP();
    b_phase<1, 1, 2>(c, r3, c.kg + (size_t)(t + 4) * RSZ, r2, t + 4 < 8, G16{});
    prev = r3;
    pos = pos + 4 >= 5 ? pos - 1 : pos + 4;
  }
  wait_bar<23>(); GAP();
  a_phase<0, 1, 1>(c, 0 * RSZ, c.vg + 7 * RSZ, 2 * RSZ, true, G16{});
  mfma_pad(); wait_bar<23>(); GAP();
  b_phase<0, 1, 3>(c, 4 * RSZ, nullptr, 0 * RSZ, false, G16{});
  wait_bar<22>(); GAP();
  a_phase<1, 1, 3>(c, 1 * RSZ, nullptr, 4 * RSZ, false, G16{});
  mfma_pad(); wait_bar<22>(); GAP();
  b_phase<1, 3, 0>(c, 0, nullptr, 0, false, G16{});
  wait_bar<14>(); GAP();
  a_phase<0, 2, 0>(c, 2 * RSZ, nullptr, 0, false, G16{});
  wait_bar<14>(); GAP();
  pv_last_(c, G16{});
  mfma_pad();

  {
    const int kh = w & 1;
    const float m = c.m;
    const float l = swap_half_sum(c.l);
    char* ex = lds + 1 * RSZ;
    float* st = (float*)(lds + 3 * RSZ + 28672);
    st[w * 128 + lane] = m;
    st[w * 128 + 64 + lane] = l;
    const unsigned dst = c.ldsa + 1 * RSZ + w * 16384 + lane * 16;
    if (kh == 0) ep_send_all<0>(dst, G16{}); else ep_send_all<1>(dst, G16{});
    {
      const int blk = (lane >> 4) & 1, qq = (lane & 15) >> 2, p = lane & 3;
      const unsigned base = c.ldsa + (4 * h + qq) * 896 + 32 * blk + 8 * p + kh * 448;
      const unsigned sw = (qq & 2) ? 64u : 0u;
      c.uaddr[0] = kh ? base - sw : base + sw;
      c.uaddr[1] = kh ? base + sw : base - sw;
    }
    wait_bar<14>();
    const int pw = w ^ 1;
    const float m2 = st[pw * 128 + lane], l2 = st[pw * 128 + 64 + lane];
    const float mn = fmaxf(m, m2);
    const float a1 = __builtin_amdgcn_exp2f((m - mn) * SM_C), a2 = __builtin_amdgcn_exp2f((m2 - mn) * SM_C);
    const float inv = 1.0f / (l * a1 + l2 * a2);
    const float f1 = a1 * inv, f2 = a2 * inv;
    char* buf = ex + pw * 16384 + lane * 16;
    if (kh == 0) ep_fin_all<0>(buf, f1, f2, make_iseq<8>{}); else ep_fin_all<1>(buf, f1, f2, make_iseq<8>{});
    wait_bar<14>(); GAP();
    const unsigned fa = c.ldsa + 1 * RSZ + (w >> 1) * 32768 + lane * 16;
    ofrag_(fa + 16384, fa, G16{});
    ufrag_(c, uslot(0) * RSZ, make_iseq<14>{});
    GAP();
  }
  c_phase<0, 7>(c); c_phase<1, 14>(c); c_phase<2, 14>(c); c_phase<3, 21>(c); c_phase<4, 14>(c); c_phase<5, 7>(c); c_phase<6, 0>(c); c_phase<7, 0>(c);
  mfma_pad();
  out_store_((char*)pws + ((size_t)((head * 16 + b * 2 + qh) * 4 + w) * 14) * 1024 + lane * 16, make_iseq<14>{});
}

__global__ __launch_bounds__(256) void mha_reduce_kernel(const __hip_bfloat16* __restrict__ pws, __hip_bfloat16* __restrict__ out) {
  const int tid = threadIdx.x, blk = blockIdx.x;
  const int tile = blk / 14, rem = (blk % 14) * 256 + tid;
  const char* src = (const char*)pws + ((size_t)tile * 3584 + rem) * 16;
  u32x4 v[16];
#pragma unroll
  for (int hh = 0; hh < 16; ++hh) v[hh] = *(const u32x4*)(src + (size_t)hh * (16 * 57344));
  float s[8] = {0.f, 0.f, 0.f, 0.f, 0.f, 0.f, 0.f, 0.f};
#pragma unroll
  for (int hh = 0; hh < 16; ++hh) {
#pragma unroll
    for (int e = 0; e < 4; ++e) {
      s[2 * e] += __uint_as_float(v[hh][e] << 16);
      s[2 * e + 1] += __uint_as_float(v[hh][e] & 0xffff0000u);
    }
  }
  const int w = rem / 896, jg = (rem % 896) >> 6, lane = tid & 63;
  const int q = tile * 64 + 32 * (w >> 1) + (lane & 31);
  const int n0 = 32 * (7 * (w & 1) + (jg >> 1)) + 16 * (jg & 1) + 4 * (lane >> 5);
  char* dst = (char*)out + (size_t)q * (DM * 2) + n0 * 2;
  u32x2 lo = {cvtpk(s[0], s[1]), cvtpk(s[2], s[3])}, hi = {cvtpk(s[4], s[5]), cvtpk(s[6], s[7])};
  *(u32x2*)dst = lo;
  *(u32x2*)(dst + 16) = hi;
}

extern "C" void kernel_launch(void* const* d_in, const int* in_sizes, int n_in, void* d_out, int out_size, void* d_ws, size_t ws_size,
                              hipStream_t stream) {
  const __hip_bfloat16* x = (const __hip_bfloat16*)d_in[0];
  const __hip_bfloat16* kc = (const __hip_bfloat16*)d_in[1];
  const __hip_bfloat16* vc = (const __hip_bfloat16*)d_in[2];
  const __hip_bfloat16* wq = (const __hip_bfloat16*)d_in[3];
  const __hip_bfloat16* wo = (const __hip_bfloat16*)d_in[4];
  __hip_bfloat16* out = (__hip_bfloat16*)d_out;
  __hip_bfloat16* pws = (__hip_bfloat16*)d_ws;
  (void)hipFuncSetAttribute((const void*)mha_attn_kernel, hipFuncAttributeMaxDynamicSharedMemorySize, K1_LDS);
  mha_attn_kernel<<<256, 256, K1_LDS, stream>>>(x, kc, vc, wq, wo, pws);
  mha_reduce_kernel<<<224, 256, 0, stream>>>(pws, out);
}
```

```cpp
#include <hip/hip_runtime.h>
#include <hip/hip_bf16.h>
#include <stdint.h>
#include <utility>

typedef __attribute__((ext_vector_type(8))) short bf16x8;
typedef __attribute__((ext_vector_type(4))) short s16x4;
typedef __attribute__((ext_vector_type(16))) float f32x16;
typedef __attribute__((ext_vector_type(4))) float f32x4;
typedef __attribute__((ext_vector_type(2))) float f32x2;
typedef __attribute__((ext_vector_type(4))) unsigned u32x4;
typedef __attribute__((ext_vector_type(2))) unsigned u32x2;
typedef __attribute__((ext_vector_type(2))) __bf16 bf16x2_t;
typedef __attribute__((address_space(3))) s16x4* lds_s16x4_ptr;

#define DI __device__ __forceinline__
#define MFMA(a, b, c) __builtin_amdgcn_mfma_f32_32x32x16_bf16((a), (b), (c), 0, 0, 0)
#define GAP() __builtin_amdgcn_sched_barrier(0)
#define ACLOB "a0","a1","a2","a3","a4","a5","a6","a7","a8","a9","a10","a11","a12","a13","a14","a15","a16","a17","a18","a19","a20","a21","a22","a23","a24","a25","a26","a27","a28","a29","a30","a31","a32","a33","a34","a35","a36","a37","a38","a39","a40","a41","a42","a43","a44","a45","a46","a47","a48","a49","a50","a51","a52","a53","a54","a55","a56","a57","a58","a59","a60","a61","a62","a63","a64","a65","a66","a67","a68","a69","a70","a71","a72","a73","a74","a75","a76","a77","a78","a79","a80","a81","a82","a83","a84","a85","a86","a87","a88","a89","a90","a91","a92","a93","a94","a95","a96","a97","a98","a99","a100","a101","a102","a103","a104","a105","a106","a107","a108","a109","a110","a111","a112","a113","a114","a115","a116","a117","a118","a119","a120","a121","a122","a123","a124","a125","a126","a127","a128","a129","a130","a131","a132","a133","a134","a135","a136","a137","a138","a139","a140","a141","a142","a143","a144","a145","a146","a147","a148","a149","a150","a151","a152","a153","a154","a155","a156","a157","a158","a159","a160","a161","a162","a163","a164","a165","a166","a167","a168","a169","a170","a171","a172","a173","a174","a175","a176","a177","a178","a179","a180","a181","a182","a183","a184","a185","a186","a187","a188","a189","a190","a191","a192","a193","a194","a195","a196","a197","a198","a199","a200","a201","a202","a203","a204","a205","a206","a207","a208","a209","a210","a211","a212","a213","a214","a215","a216","a217","a218","a219","a220","a221","a222","a223","a224","a225","a226","a227","a228","a229","a230","a231","a232","a233","a234","a235","a236","a237","a238","a239","a240","a241","a242","a243","a244","a245","a246","a247","a248","a249","a250","a251","a252","a253","a254","a255"
template <int... Is> using iseq = std::integer_sequence<int, Is...>;
template <int N> using make_iseq = std::make_integer_sequence<int, N>;

constexpr int NB = 8, SQ = 128, DM = 448, NH = 16, SK = 512, DH = 256;
constexpr int HD = NH * DH;

DI unsigned cvtpk(float lo, float hi) {
  f32x2 v = {lo, hi};
  bf16x2_t r = __builtin_convertvector(v, bf16x2_t);
  return __builtin_bit_cast(unsigned, r);
}
DI void dma16(const char* sbase, unsigned voff, unsigned ldsa) {
  asm volatile("s_mov_b32 m0, %2\n\ts_nop 0\n\tglobal_load_lds_dwordx4 %0, %1" ::"v"(voff), "s"(sbase), "s"(ldsa) : "memory");
}
DI s16x4 tr_read(const char* l) {
  return __builtin_amdgcn_ds_read_tr16_b64_v4i16((lds_s16x4_ptr)(l));
}
DI bf16x8 cat8(s16x4 a, s16x4 b) { return __builtin_shufflevector(a, b, 0, 1, 2, 3, 4, 5, 6, 7); }
template <int N> DI void wait_bar() {
  asm volatile("s_waitcnt vmcnt(%0) lgkmcnt(0)\n\ts_barrier" ::"n"(N) : "memory");
}
template <int N> DI void wait_vm() {
  asm volatile("s_waitcnt vmcnt(%0) lgkmcnt(0)" ::"n"(N) : "memory");
}
DI float swap_half_max(float v) {
  auto rr = __builtin_amdgcn_permlane32_swap(__float_as_uint(v), __float_as_uint(v), false, false);
  return fmaxf(__uint_as_float(rr[0]), __uint_as_float(rr[1]));
}
DI float swap_half_sum(float v) {
  auto rr = __builtin_amdgcn_permlane32_swap(__float_as_uint(v), __float_as_uint(v), false, false);
  return __uint_as_float(rr[0]) + __uint_as_float(rr[1]);
}
DI float max3f(float a, float b, float c) { float r; asm("v_max3_f32 %0, %1, %2, %3" : "=v"(r) : "v"(a), "v"(b), "v"(c)); return r; }
DI float max7(float p, float a, float b, float c, float d, float e, float f) {
  float r; asm("v_max3_f32 %0, %1, %2, %3\n\tv_max3_f32 %0, %0, %4, %5\n\tv_max3_f32 %0, %0, %6, %7" : "=&v"(r) : "v"(p), "v"(a), "v"(b), "v"(c), "v"(d), "v"(e), "v"(f)); return r;
}
DI void mfma_pad() { asm volatile("s_nop 7\n\ts_nop 7" ::: "memory"); }

template <int A, int OFF> DI void rd128(unsigned addr) {
  asm volatile("ds_read_b128 a[%c1:%c2], %0 offset:%c3" ::"v"(addr), "i"(A), "i"(A + 3), "i"(OFF) : "memory", ACLOB);
}
template <int A, int OFF> DI void rdtr(unsigned addr) {
  asm volatile("ds_read_b64_tr_b16 a[%c1:%c2], %0 offset:%c3" ::"v"(addr), "i"(A), "i"(A + 1), "i"(OFF) : "memory", ACLOB);
}
template <int A, int OFF0, int OFF1> DI void rdtr2(unsigned addr) {
  asm volatile("ds_read_b64_tr_b16 a[%c1:%c2], %0 offset:%c5\n\tds_read_b64_tr_b16 a[%c3:%c4], %0 offset:%c6" ::"v"(addr), "i"(A), "i"(A + 1), "i"(A + 2), "i"(A + 3), "i"(OFF0), "i"(OFF1) : "memory", ACLOB);
}
template <int A, int OFF> DI void wr128a(unsigned addr) {
  asm volatile("ds_write_b128 %0, a[%c1:%c2] offset:%c3" ::"v"(addr), "i"(A), "i"(A + 3), "i"(OFF) : "memory");
}
template <int OFF> DI void dmao(const char* sbase, unsigned voff) {
  asm volatile("global_load_lds_dwordx4 %0, %1 offset:%c2" ::"v"(voff), "s"(sbase), "i"(OFF) : "memory");
}
template <int OFF> DI void dmao_m0(const char* sbase, unsigned voff, unsigned ldsa) {
  asm volatile("s_mov_b32 m0, %2\n\ts_nop 0\n\tglobal_load_lds_dwordx4 %0, %1 offset:%c3" ::"v"(voff), "s"(sbase), "s"(ldsa), "i"(OFF) : "memory");
}
template <int D, int A, int B, bool ZERO> DI void mfma_aaa() {
  if constexpr (ZERO) asm volatile("v_mfma_f32_32x32x16_bf16 a[%c0:%c1], a[%c2:%c3], a[%c4:%c5], 0" ::"i"(D), "i"(D + 15), "i"(A), "i"(A + 3), "i"(B), "i"(B + 3) : ACLOB);
  else asm volatile("v_mfma_f32_32x32x16_bf16 a[%c0:%c1], a[%c2:%c3], a[%c4:%c5], a[%c0:%c1]" ::"i"(D), "i"(D + 15), "i"(A), "i"(A + 3), "i"(B), "i"(B + 3) : ACLOB);
}
template <int A, int B, bool ZERO> DI void mfma_vaa(f32x16& s) {
  if constexpr (ZERO) asm volatile("v_mfma_f32_32x32x16_bf16 %0, a[%c1:%c2], a[%c3:%c4], 0" : "=v"(s) : "i"(A), "i"(A + 3), "i"(B), "i"(B + 3) : ACLOB);
  else asm volatile("v_mfma_f32_32x32x16_bf16 %0, a[%c1:%c2], a[%c3:%c4], %0" : "+v"(s) : "i"(A), "i"(A + 3), "i"(B), "i"(B + 3) : ACLOB);
}
template <int D, int A, bool ZERO> DI void mfma_aav(const bf16x8& p) {
  if constexpr (ZERO) asm volatile("v_mfma_f32_32x32x16_bf16 a[%c1:%c2], a[%c3:%c4], %0, 0" ::"v"(p), "i"(D), "i"(D + 15), "i"(A), "i"(A + 3) : ACLOB);
  else asm volatile("v_mfma_f32_32x32x16_bf16 a[%c1:%c2], a[%c3:%c4], %0, a[%c1:%c2]" ::"v"(p), "i"(D), "i"(D + 15), "i"(A), "i"(A + 3) : ACLOB);
}
template <int A> DI float ard() { float r; asm volatile("v_accvgpr_read_b32 %0, a[%c1]" : "=v"(r) : "i"(A)); return r; }
template <int A> DI void ascale1(float f) {
  float t;
  asm volatile("v_accvgpr_read_b32 %0, a[%c2]\n\tv_mul_f32 %0, %1, %0\n\tv_accvgpr_write_b32 a[%c2], %0" : "=&v"(t) : "v"(f), "i"(A) : ACLOB);
}
template <int... Is> DI void ascale_all(float f, iseq<Is...>) { (ascale1<Is>(f), ...); }

constexpr int RSZ = 32768;
constexpr int K1_LDS = 5 * RSZ;
constexpr int XOFF = 3 * RSZ;
constexpr int AQ = 128, AF = 192;
constexpr float SM_C = 0.0625f * 1.4426950408889634f;
constexpr float SM_THR = 8.0f / 0.0625f;

struct K1Ctx {
  char* lds;
  unsigned ldsa;
  const char* kg;
  const char* vg;
  const char* wg;
  const char* xg;
  int w, lane;
  unsigned kaddr[8], vaddr[4], waddr[2], xaddr[4];
  unsigned kvo[8], vvo[4], wvo[8], xvo[2];
  unsigned dstw;
  const char* ug;
  unsigned uvo[7], uaddr[2], dstu;
  f32x16 S[2];
  bf16x8 P[2], P2[2];
  float m, l, alpha, mc, ps0, ps1, pm;
  bool pend;
};

template <int TYPE, int I> DI void piece(const K1Ctx& c, const char* src, unsigned slot) {
  const unsigned vo = TYPE == 0 ? c.kvo[I] : TYPE == 1 ? c.vvo[(I & 1) + 2 * (I >> 2)] : c.wvo[I];
  if constexpr ((I & 3) == 0) dmao_m0<0>(src, vo, c.dstw + slot + (I >> 2) * 4096);
  else dmao<(I & 3) * 1024>(src, vo);
}
template <int TYPE, int... Is> DI void stage_(const K1Ctx& c, const char* src, unsigned slot, iseq<Is...>) { (piece<TYPE, Is>(c, src, slot), ...); }
template <int TYPE> DI void stage_all(const K1Ctx& c, const char* src, unsigned slot) { stage_<TYPE>(c, src, slot, make_iseq<8>{}); }
template <int S> DI void xpieces(const K1Ctx& c) {
  dmao_m0<0>(c.xg + S * 128, c.xvo[0], c.ldsa + XOFF + S * 8192 + c.w * 2048);
  dmao<1024>(c.xg + S * 128, c.xvo[1]);
}
template <int S> DI void wstage(const K1Ctx& c) { xpieces<S>(c); stage_all<2>(c, c.wg + (size_t)(S * 64) * (HD * 2), (S % 3) * RSZ); }

template <int J> DI void kfrag(const K1Ctx& c, unsigned rd) { rd128<AF + 4 * J, 256 * (J >> 3)>(c.kaddr[J & 7] + rd); }
template <int J> DI void vfrag(const K1Ctx& c, unsigned rd) {
  constexpr int t = J >> 1, ks = J & 1;
  rdtr2<AF + 4 * J, ks * 8192 + 256 * (t >> 2), ks * 8192 + 256 * (t >> 2) + 4096>(c.vaddr[t & 3] + rd);
}
__host__ __device__ constexpr int qset(int s) { return (s & 1) ? AF : AQ; }
template <int S, int J> DI void wfrag(const K1Ctx& c) {
  if constexpr (J < 8) {
    constexpr int ks = J >> 1, dt = J & 1;
    rdtr2<qset(S) + 4 * J, ks * 8192, ks * 8192 + 2048>(c.waddr[dt] + (S % 3) * RSZ);
  } else {
    constexpr int ks = (J - 8) >> 1, qt = (J - 8) & 1;
    rd128<qset(S) + 4 * J, qt * 4096>(c.xaddr[ks] + S * 8192);
  }
}
__host__ __device__ constexpr int worder(int g) { return (g & 3) < 2 ? 2 * (g >> 2) + (g & 1) : 8 + 2 * (g >> 2) + (g & 1); }

template <int P, int G> DI void qp_gap(K1Ctx& c) {
  constexpr int S = P - 1, ks = G >> 2, dt = (G >> 1) & 1, qt = G & 1;
  mfma_aaa<16 * (2 * dt + qt), qset(S) + 4 * (2 * ks + dt), qset(S) + 32 + 4 * (2 * ks + qt), (S == 0 && ks == 0)>();
  if constexpr (P < 7) wfrag<P, worder(G)>(c); else kfrag<G>(c, 1 * RSZ);
  if constexpr (P <= 4) {
    if constexpr (G == 0) xpieces<P + 2>(c);
    if constexpr (G >= 1 && G < 9) piece<2, G - 1>(c, c.wg + (size_t)((P + 2) * 64) * (HD * 2), ((P + 2) % 3) * RSZ);
  } else if constexpr (P == 5) { if constexpr (G < 8) piece<0, G>(c, c.kg, 1 * RSZ); }
  else if constexpr (P == 6) { if constexpr (G < 8) piece<0, G>(c, c.kg + RSZ, 2 * RSZ); }
  else { if constexpr (G < 8) piece<1, G>(c, c.vg, 0 * RSZ); else piece<0, G - 8>(c, c.kg + 2 * RSZ, 4 * RSZ); }
  GAP();
}
template <int P, int... Gs> DI void qp_phase_(K1Ctx& c, iseq<Gs...>) { (qp_gap<P, Gs>(c), ...); }
template <int P, int N> DI void qp_phase(K1Ctx& c) { wait_bar<N>(); GAP(); qp_phase_<P>(c, make_iseq<16>{}); }
template <int... Gs> DI void wfrag0_(const K1Ctx& c, iseq<Gs...>) { (wfrag<0, worder(Gs)>(c), ...); }

template <int X, int E> DI void sm_exp(K1Ctx& c) { c.S[X][E] = __builtin_amdgcn_exp2f(fmaf(c.S[X][E], SM_C, c.mc)); }
template <int X, int E> DI void sm_add(K1Ctx& c) { if constexpr (E & 1) c.ps1 += c.S[X][E]; else c.ps0 += c.S[X][E]; }
template <int X, int KS, bool LAST = false> DI void sm_pack(K1Ctx& c) {
  u32x4 w = {cvtpk(c.S[X][8 * KS + 0], c.S[X][8 * KS + 1]), cvtpk(c.S[X][8 * KS + 2], c.S[X][8 * KS + 3]),
             cvtpk(c.S[X][8 * KS + 4], c.S[X][8 * KS + 5]), cvtpk(c.S[X][8 * KS + 6], c.S[X][8 * KS + 7])};
  if constexpr (LAST) c.P2[KS] = __builtin_bit_cast(bf16x8, w); else c.P[KS] = __builtin_bit_cast(bf16x8, w);
}
template <bool FIRST> DI void sm_decide(K1Ctx& c) {
  const float pmax = swap_half_max(c.pm);
  if constexpr (FIRST) {
    c.m = pmax;
  } else {
    if (__builtin_expect(__any(pmax - c.m > SM_THR), 0)) {
      const float mn = fmaxf(c.m, pmax);
      c.alpha = __builtin_amdgcn_exp2f((c.m - mn) * SM_C);
      c.m = mn;
      c.l *= c.alpha;
      c.pend = true;
    }
  }
  c.mc = -c.m * SM_C;
  c.ps0 = 0.f; c.ps1 = 0.f;
}

template <int I> DI void upiece(const K1Ctx& c, int s, unsigned slot) {
  const char* src = c.ug + (size_t)s * 28672;
  if constexpr ((I & 3) == 0) dmao_m0<0>(src, c.uvo[I], c.dstu + slot + (I >> 2) * 4096);
  else dmao<(I & 3) * 1024>(src, c.uvo[I]);
}
template <int G> DI void ufrag(const K1Ctx& c, unsigned slot) {
  constexpr int ksl = G / 7, j = G % 7;
  rdtr2<AF + 4 * G, ksl * 14336 + 64 * j, ksl * 14336 + 64 * j + 7168>(c.uaddr[j & 1] + slot);
}
template <int... Gs> DI void ufrag_(const K1Ctx& c, unsigned slot, iseq<Gs...>) { (ufrag<Gs>(c, slot), ...); }
__host__ __device__ constexpr int uslot(int s) { return s == 0 ? 3 : s == 1 ? 0 : s == 2 ? 4 : s == 3 ? 3 : s == 4 ? 1 : s == 5 ? 2 : s == 6 ? 0 : 4; }
template <int S, int G> DI void c_gap(K1Ctx& c, unsigned fa) {
  constexpr int ksl = G / 7, j = G % 7;
  mfma_aaa<16 * j, AF + 4 * G, AQ + 4 * (2 * S + ksl), (S == 0 && ksl == 0)>();
  ufrag<G>(c, uslot(S + 1) * RSZ);
  if constexpr (S == 0) rd128<AQ + 4 * (G + 2), 1024 * ((G + 2) & 7) + (G + 2 < 8 ? 16384 : 0)>(fa);
  if constexpr (S == 1 || S == 2) { if constexpr (G < 7) upiece<G>(c, 2 * S + 2, uslot(2 * S + 2) * RSZ); else upiece<G - 7>(c, 2 * S + 3, uslot(2 * S + 3) * RSZ); }
  GAP();
}
template <int S, int... Gs> DI void c_phase_(K1Ctx& c, unsigned fa, iseq<Gs...>) { (c_gap<S, Gs>(c, fa), ...); }
template <int S, int N> DI void c_phase(K1Ctx& c, unsigned fa) { wait_bar<N>(); GAP(); c_phase_<S>(c, fa, make_iseq<14>{}); }
template <int... Is> DI void ustage_(const K1Ctx& c, int s, unsigned slot, iseq<Is...>) { (upiece<Is>(c, s, slot), ...); }

template <int X, int KIND, int DMA, int G> DI void a_gap(K1Ctx& c, unsigned rd, const char* src, unsigned dst, bool dodma) {
  constexpr int Xp = X ^ 1;
  if constexpr (KIND != 2) mfma_vaa<AF + 4 * G, AQ + 4 * G, (G == 0)>(c.S[X]);
  if constexpr (KIND != 0) {
    vfrag<G>(c, rd);
    if constexpr (G < 8) { sm_exp<Xp, 8 + G>(c); if constexpr (G > 0) sm_add<Xp, 7 + G>(c); }
    if constexpr (G == 8) { sm_add<Xp, 15>(c); sm_pack<Xp, 0>(c); }
    if constexpr (G == 9) { sm_pack<Xp, 1>(c); c.l += c.ps0 + c.ps1; }
  }
  if constexpr (DMA == 1) { if constexpr (G >= 8) piece<1, G - 8>(c, src, dst); }
  if constexpr (DMA == 3) { if constexpr (G >= 8 && G < 15) upiece<G - 8>(c, 2, dst); }
  if constexpr (DMA == 2) {
    if constexpr (G == 10) { if (dodma) { piece<1, 0>(c, src, dst); piece<1, 1>(c, src, dst); piece<1, 2>(c, src, dst); piece<1, 3>(c, src, dst); } }
    if constexpr (G == 13) { if (dodma) { piece<1, 4>(c, src, dst); piece<1, 5>(c, src, dst); piece<1, 6>(c, src, dst); piece<1, 7>(c, src, dst); } }
  }
  GAP();
}
template <int X, int KIND, int DMA, int... Gs> DI void a_phase(K1Ctx& c, unsigned rd, const char* src, unsigned dst, bool dodma, iseq<Gs...>) {
  (a_gap<X, KIND, DMA, Gs>(c, rd, src, dst, dodma), ...);
}
template <int X, int KIND, int DMA, int G> DI void b_gap(K1Ctx& c, unsigned rd, const char* src, unsigned dst, bool dodma) {
  if constexpr (KIND != 0) mfma_aav<16 * (G >> 1), AF + 4 * G, (KIND == 2 && (G & 1) == 0)>(c.P[G & 1]);
  if constexpr (KIND != 3) kfrag<G>(c, rd);
  if constexpr (G == 0) c.pm = max7(c.S[X][0], c.S[X][1], c.S[X][2], c.S[X][3], c.S[X][4], c.S[X][5], c.S[X][6]);
  if constexpr (G == 1) c.pm = max7(c.pm, c.S[X][7], c.S[X][8], c.S[X][9], c.S[X][10], c.S[X][11], c.S[X][12]);
  if constexpr (G == 2) c.pm = max7(c.pm, c.S[X][13], c.S[X][14], c.S[X][15], c.S[X][15], c.S[X][15], c.S[X][15]);
  if constexpr (G == 3) sm_decide<KIND == 0>(c);
  if constexpr (G >= 4 && G < 12) { sm_exp<X, G - 4>(c); if constexpr (G > 4) sm_add<X, G - 5>(c); }
  if constexpr (G == 12) sm_add<X, 7>(c);
  if constexpr (DMA == 1) { if constexpr (G >= 12) { piece<0, 2 * (G - 12)>(c, src, dst); piece<0, 2 * (G - 12) + 1>(c, src, dst); } }
  if constexpr (DMA == 3) { if constexpr (G >= 9) upiece<G - 9>(c, 1, dst); }
  if constexpr (DMA == 2) {
    if constexpr (G == 12) { if (dodma) { piece<0, 0>(c, src, dst); piece<0, 1>(c, src, dst); piece<0, 2>(c, src, dst); piece<0, 3>(c, src, dst); } else { upiece<0>(c, 0, dst); upiece<1>(c, 0, dst); upiece<2>(c, 0, dst); upiece<3>(c, 0, dst); } }
    if constexpr (G == 14) { if (dodma) { piece<0, 4>(c, src, dst); piece<0, 5>(c, src, dst); piece<0, 6>(c, src, dst); piece<0, 7>(c, src, dst); } else { upiece<4>(c, 0, dst); upiece<5>(c, 0, dst); upiece<6>(c, 0, dst); } }
  }
  GAP();
}
template <int X, int KIND, int DMA, int... Gs> DI void b_phase(K1Ctx& c, unsigned rd, const char* src, unsigned dst, bool dodma, iseq<Gs...>) {
  (b_gap<X, KIND, DMA, Gs>(c, rd, src, dst, dodma), ...);
  if constexpr (KIND != 0) {
    if (__builtin_expect(c.pend, 0)) { mfma_pad(); ascale_all(c.alpha, make_iseq<128>{}); asm volatile("s_nop 3"); c.pend = false; }
  }
}
template <int KH, int I> DI void ep_send(unsigned dst) {
  wr128a<16 * (4 * (1 - KH) + (I >> 2)) + 4 * (I & 3), I * 1024>(dst);
}
template <int KH, int I> DI void pv_last_gap(K1Ctx& c, unsigned dst) {
  constexpr int G = (I + 8 * (1 - KH)) & 15;
  mfma_aav<16 * (G >> 1), AF + 4 * G, false>(c.P[G & 1]);
  if constexpr (G < 14) ufrag<G>(c, uslot(0) * RSZ);
  if constexpr (I >= 8) { ep_send<KH, 2 * (I - 8)>(dst); ep_send<KH, 2 * (I - 8) + 1>(dst); }
  GAP();
}
template <int KH, int... Is> DI void pv_last_(K1Ctx& c, unsigned dst, iseq<Is...>) { (pv_last_gap<KH, Is>(c, dst), ...); }
template <int... Gs> DI void qfrag_(unsigned (&qa)[8], iseq<Gs...>) { (rd128<AQ + 4 * Gs, 256 * (Gs >> 3)>(qa[Gs & 7]), ...); }

template <int DT, int QT, int G> DI void qimg_write(char* qi, unsigned qx) {
  constexpr int A = 16 * (2 * DT + QT) + 4 * G;
  u32x2 v = {cvtpk(ard<A>(), ard<A + 1>()), cvtpk(ard<A + 2>(), ard<A + 3>())};
  *(u32x2*)(qi + QT * 32 * 512 + (qx ^ ((4 * DT + G) << 4))) = v;
}
template <int... Is> DI void qimg_all(char* qi, unsigned qx, iseq<Is...>) { (qimg_write<(Is >> 3), ((Is >> 2) & 1), (Is & 3)>(qi, qx), ...); }

template <int A> DI unsigned fin2(float v0, float v1, f32x2 f1, f32x2 f2) {
  f32x2 a = {ard<A>(), ard<A + 1>()}, v = {v0, v1};
  f32x2 r = a * f1 + v * f2;
  return cvtpk(r[0], r[1]);
}
template <int KH, int F> DI void ep_fin(char* buf, const f32x4 (&pv)[16], f32x2 f1, f32x2 f2) {
  constexpr int t = F >> 1, i = F & 1, A = 16 * (4 * KH + t) + 8 * i;
  const f32x4 v = pv[4 * t + 2 * i], u = pv[4 * t + 2 * i + 1];
  u32x4 pk = {fin2<A>(v[0], v[1], f1, f2), fin2<A + 2>(v[2], v[3], f1, f2), fin2<A + 4>(u[0], u[1], f1, f2), fin2<A + 6>(u[2], u[3], f1, f2)};
  *(u32x4*)(buf + F * 1024) = pk;
}
template <int KH, int... Is> DI void ep_fin_all(char* buf, const f32x4 (&pv)[16], f32x2 f1, f32x2 f2, iseq<Is...>) { (ep_fin<KH, Is>(buf, pv, f1, f2), ...); }
template <int I> DI void out_store(char* dst) {
  constexpr int A = 8 * I;
  u32x4 pk = {cvtpk(ard<A>(), ard<A + 1>()), cvtpk(ard<A + 2>(), ard<A + 3>()), cvtpk(ard<A + 4>(), ard<A + 5>()), cvtpk(ard<A + 6>(), ard<A + 7>())};
  *(u32x4*)(dst + I * 1024) = pk;
}
template <int J> DI void c_last(char* dst) {
  if constexpr (J < 7) {
    mfma_aaa<16 * J, AF + 4 * J, AQ + 4 * 14, false>();
    mfma_aaa<16 * J, AF + 4 * (J + 7), AQ + 4 * 15, false>();
  } else mfma_pad();
  GAP();
  if constexpr (J > 0) { out_store<2 * (J - 1)>(dst); out_store<2 * (J - 1) + 1>(dst); }
  GAP();
}
template <int... Js> DI void c_last_(char* dst, iseq<Js...>) { (c_last<Js>(dst), ...); }

DI unsigned ring_off(int pos) { return ((0x40231u >> (4 * pos)) & 7u) << 15; }

__global__ __launch_bounds__(256, 1) void mha_attn_kernel(const __hip_bfloat16* __restrict__ x, const __hip_bfloat16* __restrict__ kc,
                                                          const __hip_bfloat16* __restrict__ vc, const __hip_bfloat16* __restrict__ wq,
                                                          const __hip_bfloat16* __restrict__ wo, __hip_bfloat16* __restrict__ pws) {
  extern __shared__ __attribute__((aligned(16))) char lds[];
  const int tid = threadIdx.x, lane = tid & 63;
  const int w = __builtin_amdgcn_readfirstlane(tid >> 6);
  const int bid = blockIdx.x;
  const int xcd = bid & 7, idx = bid >> 3;
  const int head = xcd * 2 + (idx & 1), qh = (idx >> 1) & 1, b = idx >> 2;
  const int h = lane >> 5, r = lane & 31;

  K1Ctx c;
  c.lds = lds; c.ldsa = (unsigned)(uintptr_t)lds; c.w = w; c.lane = lane;
  c.kg = (const char*)kc + (size_t)(b * NH + head) * SK * DH * 2 + w * 8192;
  c.vg = (const char*)vc + (size_t)(b * NH + head) * SK * DH * 2 + w * 8192;
  c.wg = (const char*)wq + head * DH * 2 + (size_t)(w * 16) * (HD * 2);
  c.xg = (const char*)x + (size_t)(b * SQ + qh * 64) * (DM * 2);
  c.dstw = c.ldsa + w * 8192;
#pragma unroll
  for (int i = 0; i < 8; ++i) {
    c.kvo[i] = h * 512 + ((r ^ (2 * i + h)) << 4) + (i >> 2) * 4096;
    c.wvo[i] = (2 * i + h) * (HD * 2) + ((r ^ (((2 * i + h) & 3) << 2)) << 4) - (i & 3) * 1024;
  }
#pragma unroll
  for (int i = 0; i < 4; ++i) c.vvo[i] = h * 512 + ((r ^ (((2 * (i & 1) + h) & 3) << 2)) << 4) + (i >> 1) * 4096;
#pragma unroll
  for (int i = 0; i < 2; ++i) {
    const int row = (w * 2 + i) * 8 + (lane >> 3);
    c.xvo[i] = row * (DM * 2) + (((lane & 7) ^ ((row >> 1) & 7)) << 4) - i * 1024 + 1024;
  }
  c.xg -= 1024;
  c.ug = (const char*)wo + (size_t)head * DH * DM * 2 - 1024;
  c.dstu = c.ldsa + w * 7168;
#pragma unroll
  for (int i = 0; i < 7; ++i) {
    const int p = (7 * w + i) * 64 + lane, row = p / 56, ch = p - row * 56;
    c.uvo[i] = row * 896 + ((ch ^ (((row >> 1) & 1) << 2)) << 4) - (i & 3) * 1024 + 1024;
  }
  wstage<0>(c); wstage<1>(c); wstage<2>(c);
  {
    const int blk = (lane >> 4) & 1, qq = (lane & 15) >> 2, p = lane & 3, kh = w & 1;
    const int key = 32 * kh + r;
#pragma unroll
    for (int k = 0; k < 8; ++k) c.kaddr[k] = c.ldsa + key * 512 + (((2 * k + h) ^ (key & 15)) << 4);
#pragma unroll
    for (int t = 0; t < 4; ++t) c.vaddr[t] = c.ldsa + (32 * kh + 4 * h + qq) * 512 + 32 * blk + 8 * p + 64 * (t ^ qq);
#pragma unroll
    for (int dt = 0; dt < 2; ++dt) c.waddr[dt] = c.ldsa + (8 * h + qq) * 512 + 64 * ((2 * w + dt) ^ qq) + 32 * blk + 8 * p;
#pragma unroll
    for (int ks = 0; ks < 4; ++ks) c.xaddr[ks] = c.ldsa + XOFF + r * 128 + (((2 * ks + h) ^ ((r >> 1) & 7)) << 4);
  }
  c.pend = false; c.l = 0.f; c.alpha = 1.f; c.m = 0.f; c.ps0 = 0.f; c.ps1 = 0.f; c.pm = 0.f; c.mc = 0.f;

  wait_bar<20>(); GAP();
  wfrag0_(c, make_iseq<16>{}); GAP();
  qp_phase<1, 10>(c); qp_phase<2, 10>(c); qp_phase<3, 10>(c); qp_phase<4, 10>(c); qp_phase<5, 10>(c); qp_phase<6, 8>(c); qp_phase<7, 8>(c);
  mfma_pad();
  qimg_all(lds + 3 * RSZ, (unsigned)(r * 512 + 8 * h + (((r & 15) ^ (8 * w)) << 4)), make_iseq<16>{});
  wait_bar<32>(); GAP();
  {
    const int q = 32 * (w >> 1) + r;
    unsigned qa[8];
#pragma unroll
    for (int k = 0; k < 8; ++k) qa[k] = c.ldsa + 3 * RSZ + q * 512 + (((2 * k + h) ^ (q & 15)) << 4);
    qfrag_(qa, make_iseq<16>{});
  }
  wait_vm<32>(); GAP();

  typedef make_iseq<16> G16;
  a_phase<0, 0, 1>(c, 0, c.vg + RSZ, 1 * RSZ, true, G16{});
  mfma_pad(); wait_bar<24>(); GAP();
  b_phase<0, 0, 1>(c, 2 * RSZ, c.kg + 3 * RSZ, 3 * RSZ, true, G16{});
  wait_bar<24>(); GAP();
  a_phase<1, 1, 1>(c, 0 * RSZ, c.vg + 2 * RSZ, 2 * RSZ, true, G16{});
  mfma_pad(); wait_bar<24>(); GAP();
  b_phase<1, 2, 1>(c, 4 * RSZ, c.kg + 4 * RSZ, 0 * RSZ, true, G16{});
  int pos = 0;
  unsigned prev = 4 * RSZ;
  for (int t = 2; t < 6; t += 2) {
    unsigned r0 = ring_off(pos), r1 = ring_off(pos + 1 >= 5 ? pos - 4 : pos + 1), r2 = ring_off(pos + 2 >= 5 ? pos - 3 : pos + 2), r3 = ring_off(pos + 3 >= 5 ? pos - 2 : pos + 3);
    wait_bar<24>(); GAP();
    a_phase<0, 1, 1>(c, r0, c.vg + (size_t)(t + 1) * RSZ, prev, true, G16{});
    mfma_pad(); wait_bar<24>(); GAP();
    b_phase<0, 1, 1>(c, r1, c.kg + (size_t)(t + 3) * RSZ, r0, true, G16{});
    wait_bar<24>(); GAP();
    a_phase<1, 1, 1>(c, r2, c.vg + (size_t)(t + 2) * RSZ, r1, true, G16{});
    mfma_pad(); wait_bar<24>(); GAP();
    b_phase<1, 1, 2>(c, r3, c.kg + (size_t)(t + 4) * RSZ, r2, t + 4 < 8, G16{});
    prev = r3;
    pos = pos + 4 >= 5 ? pos - 1 : pos + 4;
  }
  wait_bar<23>(); GAP();
  a_phase<0, 1, 1>(c, 0 * RSZ, c.vg + 7 * RSZ, 2 * RSZ, true, G16{});
  mfma_pad(); wait_bar<23>(); GAP();
  b_phase<0, 1, 3>(c, 4 * RSZ, nullptr, 0 * RSZ, false, G16{});
  wait_bar<22>(); GAP();
  a_phase<1, 1, 3>(c, 1 * RSZ, nullptr, 4 * RSZ, false, G16{});
  mfma_pad(); wait_bar<22>(); GAP();
  b_phase<1, 3, 0>(c, 0, nullptr, 0, false, G16{});
  wait_bar<14>(); GAP();
  a_phase<0, 2, 0>(c, 2 * RSZ, nullptr, 0, false, G16{});
  {
    const int kh = w & 1, pw = w ^ 1;
    const float m = c.m;
    const float l = swap_half_sum(c.l);
    float* st = (float*)(lds + 3 * RSZ + 28672);
    st[w * 128 + lane] = m;
    st[w * 128 + 64 + lane] = l;
    {
      const int blk = (lane >> 4) & 1, qq = (lane & 15) >> 2, p = lane & 3;
      const unsigned base = c.ldsa + (4 * h + qq) * 896 + 32 * blk + 8 * p + kh * 448;
      const unsigned sw = (qq & 2) ? 64u : 0u;
      c.uaddr[0] = kh ? base - sw : base + sw;
      c.uaddr[1] = kh ? base + sw : base - sw;
    }
    wait_bar<14>(); GAP();
    const float m2 = st[pw * 128 + lane], l2 = st[pw * 128 + 64 + lane];
    const unsigned dst = c.ldsa + 1 * RSZ + w * 16384 + lane * 16;
    if (kh == 0) pv_last_<0>(c, dst, G16{}); else pv_last_<1>(c, dst, G16{});
    const float mn = fmaxf(m, m2);
    const float a1 = __builtin_amdgcn_exp2f((m - mn) * SM_C), a2 = __builtin_amdgcn_exp2f((m2 - mn) * SM_C);
    const float inv = 1.0f / (l * a1 + l2 * a2);
    const f32x2 f1 = {a1 * inv, a1 * inv}, f2 = {a2 * inv, a2 * inv};
    wait_bar<14>(); GAP();
    ustage_(c, 3, uslot(3) * RSZ, make_iseq<7>{});
    char* buf = lds + 1 * RSZ + pw * 16384 + lane * 16;
    f32x4 pv[16];
#pragma unroll
    for (int i = 0; i < 16; ++i) pv[i] = *(const f32x4*)(buf + i * 1024);
    if (kh == 0) ep_fin_all<0>(buf, pv, f1, f2, make_iseq<8>{}); else ep_fin_all<1>(buf, pv, f1, f2, make_iseq<8>{});
  }
  wait_bar<21>(); GAP();
  {
    const unsigned fa = c.ldsa + 1 * RSZ + (w >> 1) * 32768 + lane * 16;
    rd128<AQ, 16384>(fa); rd128<AQ + 4, 16384 + 1024>(fa);
    c_phase<0, 14>(c, fa); c_phase<1, 7>(c, fa); c_phase<2, 14>(c, fa); c_phase<3, 21>(c, fa); c_phase<4, 14>(c, fa); c_phase<5, 7>(c, fa); c_phase<6, 0>(c, fa);
    wait_vm<0>(); GAP();
    c_last_((char*)pws + ((size_t)((head * 16 + b * 2 + qh) * 4 + w) * 14) * 1024 + lane * 16, make_iseq<8>{});
  }
}

__global__ __launch_bounds__(256) void mha_reduce_kernel(const __hip_bfloat16* __restrict__ pws, __hip_bfloat16* __restrict__ out) {
  const int tid = threadIdx.x, blk = blockIdx.x;
  const int tile = blk / 14, rem = (blk % 14) * 256 + tid;
  const char* src = (const char*)pws + ((size_t)tile * 3584 + rem) * 16;
  u32x4 v[16];
#pragma unroll
  for (int hh = 0; hh < 16; ++hh) v[hh] = *(const u32x4*)(src + (size_t)hh * (16 * 57344));
  float s[8] = {0.f, 0.f, 0.f, 0.f, 0.f, 0.f, 0.f, 0.f};
#pragma unroll
  for (int hh = 0; hh < 16; ++hh) {
#pragma unroll
    for (int e = 0; e < 4; ++e) {
      s[2 * e] += __uint_as_float(v[hh][e] << 16);
      s[2 * e + 1] += __uint_as_float(v[hh][e] & 0xffff0000u);
    }
  }
  const int w = rem / 896, jg = (rem % 896) >> 6, lane = tid & 63;
  const int q = tile * 64 + 32 * (w >> 1) + (lane & 31);
  const int n0 = 32 * (7 * (w & 1) + (jg >> 1)) + 16 * (jg & 1) + 4 * (lane >> 5);
  char* dst = (char*)out + (size_t)q * (DM * 2) + n0 * 2;
  u32x2 lo = {cvtpk(s[0], s[1]), cvtpk(s[2], s[3])}, hi = {cvtpk(s[4], s[5]), cvtpk(s[6], s[7])};
  *(u32x2*)dst = lo;
  *(u32x2*)(dst + 16) = hi;
}

extern "C" void kernel_launch(void* const* d_in, const int* in_sizes, int n_in, void* d_out, int out_size, void* d_ws, size_t ws_size,
                              hipStream_t stream) {
  const __hip_bfloat16* x = (const __hip_bfloat16*)d_in[0];
  const __hip_bfloat16* kc = (const __hip_bfloat16*)d_in[1];
  const __hip_bfloat16* vc = (const __hip_bfloat16*)d_in[2];
  const __hip_bfloat16* wq = (const __hip_bfloat16*)d_in[3];
  const __hip_bfloat16* wo = (const __hip_bfloat16*)d_in[4];
  __hip_bfloat16* out = (__hip_bfloat16*)d_out;
  __hip_bfloat16* pws = (__hip_bfloat16*)d_ws;
  (void)hipFuncSetAttribute((const void*)mha_attn_kernel, hipFuncAttributeMaxDynamicSharedMemorySize, K1_LDS);
  mha_attn_kernel<<<256, 256, K1_LDS, stream>>>(x, kc, vc, wq, wo, pws);
  mha_reduce_kernel<<<224, 256, 0, stream>>>(pws, out);
}
```

```cpp
#include <hip/hip_runtime.h>
#include <hip/hip_bf16.h>
#include <stdint.h>
#include <utility>

typedef __attribute__((ext_vector_type(8))) short bf16x8;
typedef __attribute__((ext_vector_type(4))) short s16x4;
typedef __attribute__((ext_vector_type(16))) float f32x16;
typedef __attribute__((ext_vector_type(4))) float f32x4;
typedef __attribute__((ext_vector_type(2))) float f32x2;
typedef __attribute__((ext_vector_type(4))) unsigned u32x4;
typedef __attribute__((ext_vector_type(2))) unsigned u32x2;
typedef __attribute__((ext_vector_type(2))) __bf16 bf16x2_t;
typedef __attribute__((address_space(3))) s16x4* lds_s16x4_ptr;

#define DI __device__ __forceinline__
#define MFMA(a, b, c) __builtin_amdgcn_mfma_f32_32x32x16_bf16((a), (b), (c), 0, 0, 0)
#define GAP() __builtin_amdgcn_sched_barrier(0)
#define ACLOB "a0","a1","a2","a3","a4","a5","a6","a7","a8","a9","a10","a11","a12","a13","a14","a15","a16","a17","a18","a19","a20","a21","a22","a23","a24","a25","a26","a27","a28","a29","a30","a31","a32","a33","a34","a35","a36","a37","a38","a39","a40","a41","a42","a43","a44","a45","a46","a47","a48","a49","a50","a51","a52","a53","a54","a55","a56","a57","a58","a59","a60","a61","a62","a63","a64","a65","a66","a67","a68","a69","a70","a71","a72","a73","a74","a75","a76","a77","a78","a79","a80","a81","a82","a83","a84","a85","a86","a87","a88","a89","a90","a91","a92","a93","a94","a95","a96","a97","a98","a99","a100","a101","a102","a103","a104","a105","a106","a107","a108","a109","a110","a111","a112","a113","a114","a115","a116","a117","a118","a119","a120","a121","a122","a123","a124","a125","a126","a127","a128","a129","a130","a131","a132","a133","a134","a135","a136","a137","a138","a139","a140","a141","a142","a143","a144","a145","a146","a147","a148","a149","a150","a151","a152","a153","a154","a155","a156","a157","a158","a159","a160","a161","a162","a163","a164","a165","a166","a167","a168","a169","a170","a171","a172","a173","a174","a175","a176","a177","a178","a179","a180","a181","a182","a183","a184","a185","a186","a187","a188","a189","a190","a191","a192","a193","a194","a195","a196","a197","a198","a199","a200","a201","a202","a203","a204","a205","a206","a207","a208","a209","a210","a211","a212","a213","a214","a215","a216","a217","a218","a219","a220","a221","a222","a223","a224","a225","a226","a227","a228","a229","a230","a231","a232","a233","a234","a235","a236","a237","a238","a239","a240","a241","a242","a243","a244","a245","a246","a247","a248","a249","a250","a251","a252","a253","a254","a255"
template <int... Is> using iseq = std::integer_sequence<int, Is...>;
template <int N> using make_iseq = std::make_integer_sequence<int, N>;

constexpr int NB = 8, SQ = 128, DM = 448, NH = 16, SK = 512, DH = 256;
constexpr int HD = NH * DH;

DI unsigned cvtpk(float lo, float hi) {
  f32x2 v = {lo, hi};
  bf16x2_t r = __builtin_convertvector(v, bf16x2_t);
  return __builtin_bit_cast(unsigned, r);
}
DI void dma16(const char* sbase, unsigned voff, unsigned ldsa) {
  asm volatile("s_mov_b32 m0, %2\n\ts_nop 0\n\tglobal_load_lds_dwordx4 %0, %1" ::"v"(voff), "s"(sbase), "s"(ldsa) : "memory");
}
DI s16x4 tr_read(const char* l) {
  return __builtin_amdgcn_ds_read_tr16_b64_v4i16((lds_s16x4_ptr)(l));
}
DI bf16x8 cat8(s16x4 a, s16x4 b) { return __builtin_shufflevector(a, b, 0, 1, 2, 3, 4, 5, 6, 7); }
template <int N> DI void wait_bar() {
  asm volatile("s_waitcnt vmcnt(%0) lgkmcnt(0)\n\ts_barrier" ::"n"(N) : "memory");
}
template <int N> DI void wait_vm() {
  asm volatile("s_waitcnt vmcnt(%0) lgkmcnt(0)" ::"n"(N) : "memory");
}
DI float swap_half_max(float v) {
  auto rr = __builtin_amdgcn_permlane32_swap(__float_as_uint(v), __float_as_uint(v), false, false);
  return fmaxf(__uint_as_float(rr[0]), __uint_as_float(rr[1]));
}
DI float swap_half_sum(float v) {
  auto rr = __builtin_amdgcn_permlane32_swap(__float_as_uint(v), __float_as_uint(v), false, false);
  return __uint_as_float(rr[0]) + __uint_as_float(rr[1]);
}
DI float max3f(float a, float b, float c) { float r; asm("v_max3_f32 %0, %1, %2, %3" : "=v"(r) : "v"(a), "v"(b), "v"(c)); return r; }
DI float max7(float p, float a, float b, float c, float d, float e, float f) {
  float r; asm("v_max3_f32 %0, %1, %2, %3\n\tv_max3_f32 %0, %0, %4, %5\n\tv_max3_f32 %0, %0, %6, %7" : "=&v"(r) : "v"(p), "v"(a), "v"(b), "v"(c), "v"(d), "v"(e), "v"(f)); return r;
}
DI void mfma_pad() { asm volatile("s_nop 7\n\ts_nop 7" ::: "memory"); }

template <int A, int OFF> DI void rd128(unsigned addr) {
  asm volatile("ds_read_b128 a[%c1:%c2], %0 offset:%c3" ::"v"(addr), "i"(A), "i"(A + 3), "i"(OFF) : "memory", ACLOB);
}
template <int A, int OFF> DI void rdtr(unsigned addr) {
  asm volatile("ds_read_b64_tr_b16 a[%c1:%c2], %0 offset:%c3" ::"v"(addr), "i"(A), "i"(A + 1), "i"(OFF) : "memory", ACLOB);
}
template <int A, int OFF0, int OFF1> DI void rdtr2(unsigned addr) {
  asm volatile("ds_read_b64_tr_b16 a[%c1:%c2], %0 offset:%c5\n\tds_read_b64_tr_b16 a[%c3:%c4], %0 offset:%c6" ::"v"(addr), "i"(A), "i"(A + 1), "i"(A + 2), "i"(A + 3), "i"(OFF0), "i"(OFF1) : "memory", ACLOB);
}
template <int A, int OFF> DI void wr128a(unsigned addr) {
  asm volatile("ds_write_b128 %0, a[%c1:%c2] offset:%c3" ::"v"(addr), "i"(A), "i"(A + 3), "i"(OFF) : "memory");
}
template <int OFF> DI void dmao(const char* sbase, unsigned voff) {
  asm volatile("global_load_lds_dwordx4 %0, %1 offset:%c2" ::"v"(voff), "s"(sbase), "i"(OFF) : "memory");
}
template <int OFF> DI void dmao_m0(const char* sbase, unsigned voff, unsigned ldsa) {
  asm volatile("s_mov_b32 m0, %2\n\ts_nop 0\n\tglobal_load_lds_dwordx4 %0, %1 offset:%c3" ::"v"(voff), "s"(sbase), "s"(ldsa), "i"(OFF) : "memory");
}
template <int D, int A, int B, bool ZERO> DI void mfma_aaa() {
  if constexpr (ZERO) asm volatile("v_mfma_f32_32x32x16_bf16 a[%c0:%c1], a[%c2:%c3], a[%c4:%c5], 0" ::"i"(D), "i"(D + 15), "i"(A), "i"(A + 3), "i"(B), "i"(B + 3) : ACLOB);
  else asm volatile("v_mfma_f32_32x32x16_bf16 a[%c0:%c1], a[%c2:%c3], a[%c4:%c5], a[%c0:%c1]" ::"i"(D), "i"(D + 15), "i"(A), "i"(A + 3), "i"(B), "i"(B + 3) : ACLOB);
}
template <int A, int B, bool ZERO> DI void mfma_vaa(f32x16& s) {
  if constexpr (ZERO) asm volatile("v_mfma_f32_32x32x16_bf16 %0, a[%c1:%c2], a[%c3:%c4], 0" : "=v"(s) : "i"(A), "i"(A + 3), "i"(B), "i"(B + 3) : ACLOB);
  else asm volatile("v_mfma_f32_32x32x16_bf16 %0, a[%c1:%c2], a[%c3:%c4], %0" : "+v"(s) : "i"(A), "i"(A + 3), "i"(B), "i"(B + 3) : ACLOB);
}
template <int D, int A, bool ZERO> DI void mfma_aav(const bf16x8& p) {
  if constexpr (ZERO) asm volatile("v_mfma_f32_32x32x16_bf16 a[%c1:%c2], a[%c3:%c4], %0, 0" ::"v"(p), "i"(D), "i"(D + 15), "i"(A), "i"(A + 3) : ACLOB);
  else asm volatile("v_mfma_f32_32x32x16_bf16 a[%c1:%c2], a[%c3:%c4], %0, a[%c1:%c2]" ::"v"(p), "i"(D), "i"(D + 15), "i"(A), "i"(A + 3) : ACLOB);
}
template <int A> DI float ard() { float r; asm volatile("v_accvgpr_read_b32 %0, a[%c1]" : "=v"(r) : "i"(A)); return r; }
template <int A> DI void ascale1(float f) {
  float t;
  asm volatile("v_accvgpr_read_b32 %0, a[%c2]\n\tv_mul_f32 %0, %1, %0\n\tv_accvgpr_write_b32 a[%c2], %0" : "=&v"(t) : "v"(f), "i"(A) : ACLOB);
}
template <int... Is> DI void ascale_all(float f, iseq<Is...>) { (ascale1<Is>(f), ...); }

constexpr int RSZ = 32768;
constexpr int K1_LDS = 5 * RSZ;
constexpr int XOFF = 3 * RSZ;
constexpr int AQ = 128, AF = 192;
constexpr float SM_C = 0.0625f * 1.4426950408889634f;
constexpr float SM_THR = 8.0f / 0.0625f;

struct K1Ctx {
  char* lds;
  unsigned ldsa;
  const char* kg;
  const char* vg;
  const char* wg;
  const char* xg;
  int w, lane;
  unsigned kaddr[8], vaddr[4], waddr[2], xaddr[4];
  unsigned kvo[8], vvo[4], wvo[8], xvo[2];
  unsigned dstw;
  const char* ug;
  unsigned uvo[7], uaddr[2], dstu;
  f32x16 S[2];
  bf16x8 P[2], P2[2];
  float m, l, alpha, mc, ps0, ps1, pm;
  bool pend;
};

template <int TYPE, int I> DI void piece(const K1Ctx& c, const char* src, unsigned slot) {
  const unsigned vo = TYPE == 0 ? c.kvo[I] : TYPE == 1 ? c.vvo[(I & 1) + 2 * (I >> 2)] : c.wvo[I];
  if constexpr ((I & 3) == 0) dmao_m0<0>(src, vo, c.dstw + slot + (I >> 2) * 4096);
  else dmao<(I & 3) * 1024>(src, vo);
}
template <int TYPE, int... Is> DI void stage_(const K1Ctx& c, const char* src, unsigned slot, iseq<Is...>) { (piece<TYPE, Is>(c, src, slot), ...); }
template <int TYPE> DI void stage_all(const K1Ctx& c, const char* src, unsigned slot) { stage_<TYPE>(c, src, slot, make_iseq<8>{}); }
template <int S> DI void xpieces(const K1Ctx& c) {
  dmao_m0<0>(c.xg + S * 128, c.xvo[0], c.ldsa + XOFF + (S & 3) * 8192 + c.w * 2048);
  dmao<1024>(c.xg + S * 128, c.xvo[1]);
}
template <int S> DI void wstage(const K1Ctx& c) { xpieces<S>(c); stage_all<2>(c, c.wg + (size_t)(S * 64) * (HD * 2), (S % 3) * RSZ); }

template <int J> DI void kfrag(const K1Ctx& c, unsigned rd) { rd128<AF + 4 * J, 256 * (J >> 3)>(c.kaddr[J & 7] + rd); }
template <int J> DI void vfrag(const K1Ctx& c, unsigned rd) {
  constexpr int t = J >> 1, ks = J & 1;
  rdtr2<AF + 4 * J, ks * 8192 + 256 * (t >> 2), ks * 8192 + 256 * (t >> 2) + 4096>(c.vaddr[t & 3] + rd);
}
__host__ __device__ constexpr int qset(int s) { return (s & 1) ? AF : AQ; }
template <int S, int J> DI void wfrag(const K1Ctx& c) {
  if constexpr (J < 8) {
    constexpr int ks = J >> 1, dt = J & 1;
    rdtr2<qset(S) + 4 * J, ks * 8192, ks * 8192 + 2048>(c.waddr[dt] + (S % 3) * RSZ);
  } else {
    constexpr int ks = (J - 8) >> 1, qt = (J - 8) & 1;
    rd128<qset(S) + 4 * J, qt * 4096>(c.xaddr[ks] + (S & 3) * 8192);
  }
}
__host__ __device__ constexpr int worder(int g) { return (g & 3) < 2 ? 2 * (g >> 2) + (g & 1) : 8 + 2 * (g >> 2) + (g & 1); }

template <int P, int G> DI void qp_gap(K1Ctx& c) {
  constexpr int S = P - 1, ks = G >> 2, dt = (G >> 1) & 1, qt = G & 1;
  mfma_aaa<16 * (2 * dt + qt), qset(S) + 4 * (2 * ks + dt), qset(S) + 32 + 4 * (2 * ks + qt), (S == 0 && ks == 0)>();
  if constexpr (P < 7) wfrag<P, worder(G)>(c); else kfrag<G>(c, 4 * RSZ);
  if constexpr (P <= 4) {
    if constexpr (G == 0) xpieces<P + 2>(c);
    if constexpr (G >= 1 && G < 9) piece<2, G - 1>(c, c.wg + (size_t)((P + 2) * 64) * (HD * 2), ((P + 2) % 3) * RSZ);
  } else if constexpr (P == 5) { if constexpr (G < 8) piece<0, G>(c, c.kg + RSZ, 1 * RSZ); }
  else if constexpr (P == 6) { if constexpr (G < 8) piece<1, G>(c, c.vg, 2 * RSZ); }
  else { if constexpr (G < 8) piece<0, G>(c, c.kg + 2 * RSZ, 0 * RSZ); }
  GAP();
}
template <int P, int... Gs> DI void qp_phase_(K1Ctx& c, iseq<Gs...>) { (qp_gap<P, Gs>(c), ...); }
template <int P, int N> DI void qp_phase(K1Ctx& c) { wait_bar<N>(); GAP(); qp_phase_<P>(c, make_iseq<16>{}); }
template <int... Gs> DI void wfrag0_(const K1Ctx& c, iseq<Gs...>) { (wfrag<0, worder(Gs)>(c), ...); }

template <int X, int E> DI void sm_exp(K1Ctx& c) { c.S[X][E] = __builtin_amdgcn_exp2f(fmaf(c.S[X][E], SM_C, c.mc)); }
template <int X, int E> DI void sm_add(K1Ctx& c) { if constexpr (E & 1) c.ps1 += c.S[X][E]; else c.ps0 += c.S[X][E]; }
template <int X, int KS, bool LAST = false> DI void sm_pack(K1Ctx& c) {
  u32x4 w = {cvtpk(c.S[X][8 * KS + 0], c.S[X][8 * KS + 1]), cvtpk(c.S[X][8 * KS + 2], c.S[X][8 * KS + 3]),
             cvtpk(c.S[X][8 * KS + 4], c.S[X][8 * KS + 5]), cvtpk(c.S[X][8 * KS + 6], c.S[X][8 * KS + 7])};
  if constexpr (LAST) c.P2[KS] = __builtin_bit_cast(bf16x8, w); else c.P[KS] = __builtin_bit_cast(bf16x8, w);
}
template <bool FIRST> DI void sm_decide(K1Ctx& c) {
  const float pmax = swap_half_max(c.pm);
  if constexpr (FIRST) {
    c.m = pmax;
  } else {
    if (__builtin_expect(__any(pmax - c.m > SM_THR), 0)) {
      const float mn = fmaxf(c.m, pmax);
      c.alpha = __builtin_amdgcn_exp2f((c.m - mn) * SM_C);
      c.m = mn;
      c.l *= c.alpha;
      c.pend = true;
    }
  }
  c.mc = -c.m * SM_C;
  c.ps0 = 0.f; c.ps1 = 0.f;
}

template <int I> DI void upiece(const K1Ctx& c, int s, unsigned slot) {
  const char* src = c.ug + (size_t)s * 28672;
  if constexpr ((I & 3) == 0) dmao_m0<0>(src, c.uvo[I], c.dstu + slot + (I >> 2) * 4096);
  else dmao<(I & 3) * 1024>(src, c.uvo[I]);
}
template <int G> DI void ufrag(const K1Ctx& c, unsigned slot) {
  constexpr int ksl = G / 7, j = G % 7;
  rdtr2<AF + 4 * G, ksl * 14336 + 64 * j, ksl * 14336 + 64 * j + 7168>(c.uaddr[j & 1] + slot);
}
template <int... Gs> DI void ufrag_(const K1Ctx& c, unsigned slot, iseq<Gs...>) { (ufrag<Gs>(c, slot), ...); }
__host__ __device__ constexpr int uslot(int s) { return s == 0 ? 3 : s == 1 ? 2 : s == 2 ? 0 : s == 3 ? 3 : s == 4 ? 4 : s == 5 ? 1 : s == 6 ? 2 : 0; }
template <int S, int G> DI void c_gap(K1Ctx& c, unsigned fa) {
  constexpr int ksl = G / 7, j = G % 7;
  mfma_aaa<16 * j, AF + 4 * G, AQ + 4 * (2 * S + ksl), (S == 0 && ksl == 0)>();
  ufrag<G>(c, uslot(S + 1) * RSZ);
  if constexpr (S == 0) rd128<AQ + 4 * (G + 2), 1024 * ((G + 2) & 7) + (G + 2 < 8 ? 16384 : 0)>(fa);
  if constexpr (S == 1 || S == 2) { if constexpr (G < 7) upiece<G>(c, 2 * S + 2, uslot(2 * S + 2) * RSZ); else upiece<G - 7>(c, 2 * S + 3, uslot(2 * S + 3) * RSZ); }
  GAP();
}
template <int S, int... Gs> DI void c_phase_(K1Ctx& c, unsigned fa, iseq<Gs...>) { (c_gap<S, Gs>(c, fa), ...); }
template <int S, int N> DI void c_phase(K1Ctx& c, unsigned fa) { wait_bar<N>(); GAP(); c_phase_<S>(c, fa, make_iseq<14>{}); }
template <int... Is> DI void ustage_(const K1Ctx& c, int s, unsigned slot, iseq<Is...>) { (upiece<Is>(c, s, slot), ...); }

template <int X, int KIND, int DMA, int G> DI void a_gap(K1Ctx& c, unsigned rd, const char* src, unsigned dst, bool dodma) {
  constexpr int Xp = X ^ 1;
  if constexpr (KIND != 2) mfma_vaa<AF + 4 * G, AQ + 4 * G, (G == 0)>(c.S[X]);
  if constexpr (KIND != 0) {
    vfrag<G>(c, rd);
    if constexpr (G < 8) { sm_exp<Xp, 8 + G>(c); if constexpr (G > 0) sm_add<Xp, 7 + G>(c); }
    if constexpr (G == 8) { sm_add<Xp, 15>(c); sm_pack<Xp, 0>(c); }
    if constexpr (G == 9) { sm_pack<Xp, 1>(c); c.l += c.ps0 + c.ps1; }
  }
  if constexpr (DMA == 1) { if constexpr (G >= 8) piece<1, G - 8>(c, src, dst); }
  if constexpr (DMA == 3) { if constexpr (G >= 8 && G < 15) upiece<G - 8>(c, 2, dst); }
  if constexpr (DMA == 2) {
    if constexpr (G == 10) { if (dodma) { piece<1, 0>(c, src, dst); piece<1, 1>(c, src, dst); piece<1, 2>(c, src, dst); piece<1, 3>(c, src, dst); } }
    if constexpr (G == 13) { if (dodma) { piece<1, 4>(c, src, dst); piece<1, 5>(c, src, dst); piece<1, 6>(c, src, dst); piece<1, 7>(c, src, dst); } }
  }
  GAP();
}
template <int X, int KIND, int DMA, int... Gs> DI void a_phase(K1Ctx& c, unsigned rd, const char* src, unsigned dst, bool dodma, iseq<Gs...>) {
  (a_gap<X, KIND, DMA, Gs>(c, rd, src, dst, dodma), ...);
}
template <int X, int KIND, int DMA, int G> DI void b_gap(K1Ctx& c, unsigned rd, const char* src, unsigned dst, bool dodma) {
  if constexpr (KIND != 0) mfma_aav<16 * (G >> 1), AF + 4 * G, (KIND == 2 && (G & 1) == 0)>(c.P[G & 1]);
  if constexpr (KIND != 3) kfrag<G>(c, rd);
  if constexpr (G == 0) c.pm = max7(c.S[X][0], c.S[X][1], c.S[X][2], c.S[X][3], c.S[X][4], c.S[X][5], c.S[X][6]);
  if constexpr (G == 1) c.pm = max7(c.pm, c.S[X][7], c.S[X][8], c.S[X][9], c.S[X][10], c.S[X][11], c.S[X][12]);
  if constexpr (G == 2) c.pm = max7(c.pm, c.S[X][13], c.S[X][14], c.S[X][15], c.S[X][15], c.S[X][15], c.S[X][15]);
  if constexpr (G == 3) sm_decide<KIND == 0>(c);
  if constexpr (G >= 4 && G < 12) { sm_exp<X, G - 4>(c); if constexpr (G > 4) sm_add<X, G - 5>(c); }
  if constexpr (G == 12) sm_add<X, 7>(c);
  if constexpr (DMA == 1) { if constexpr (G >= 12) { piece<0, 2 * (G - 12)>(c, src, dst); piece<0, 2 * (G - 12) + 1>(c, src, dst); } }
  if constexpr (DMA == 3) { if constexpr (G >= 9) upiece<G - 9>(c, 1, dst); }
  if constexpr (DMA == 2) {
    if constexpr (G == 12) { if (dodma) { piece<0, 0>(c, src, dst); piece<0, 1>(c, src, dst); piece<0, 2>(c, src, dst); piece<0, 3>(c, src, dst); } else { upiece<0>(c, 0, dst); upiece<1>(c, 0, dst); upiece<2>(c, 0, dst); upiece<3>(c, 0, dst); } }
    if constexpr (G == 14) { if (dodma) { piece<0, 4>(c, src, dst); piece<0, 5>(c, src, dst); piece<0, 6>(c, src, dst); piece<0, 7>(c, src, dst); } else { upiece<4>(c, 0, dst); upiece<5>(c, 0, dst); upiece<6>(c, 0, dst); } }
  }
  GAP();
}
template <int X, int KIND, int DMA, int... Gs> DI void b_phase(K1Ctx& c, unsigned rd, const char* src, unsigned dst, bool dodma, iseq<Gs...>) {
  (b_gap<X, KIND, DMA, Gs>(c, rd, src, dst, dodma), ...);
  if constexpr (KIND != 0) {
    if (__builtin_expect(c.pend, 0)) { mfma_pad(); ascale_all(c.alpha, make_iseq<128>{}); asm volatile("s_nop 3"); c.pend = false; }
  }
}
template <int KH, int I> DI void ep_send(unsigned dst) {
  wr128a<16 * (4 * (1 - KH) + (I >> 2)) + 4 * (I & 3), I * 1024>(dst);
}
template <int KH, int I> DI void pv_last_gap(K1Ctx& c, unsigned dst) {
  constexpr int G = (I + 8 * (1 - KH)) & 15;
  mfma_aav<16 * (G >> 1), AF + 4 * G, false>(c.P[G & 1]);
  if constexpr (G < 14) ufrag<G>(c, uslot(0) * RSZ);
  if constexpr (I >= 8) { ep_send<KH, 2 * (I - 8)>(dst); ep_send<KH, 2 * (I - 8) + 1>(dst); }
  GAP();
}
template <int KH, int... Is> DI void pv_last_(K1Ctx& c, unsigned dst, iseq<Is...>) { (pv_last_gap<KH, Is>(c, dst), ...); }
template <int... Gs> DI void qfrag_(unsigned (&qa)[8], iseq<Gs...>) { (rd128<AQ + 4 * Gs, 256 * (Gs >> 3)>(qa[Gs & 7]), ...); }

template <int DT, int QT, int G> DI void qimg_write(char* qi, unsigned qx) {
  constexpr int A = 16 * (2 * DT + QT) + 4 * G;
  u32x2 v = {cvtpk(ard<A>(), ard<A + 1>()), cvtpk(ard<A + 2>(), ard<A + 3>())};
  *(u32x2*)(qi + QT * 32 * 512 + (qx ^ ((4 * DT + G) << 4))) = v;
}
template <int... Is> DI void qimg_all(char* qi, unsigned qx, iseq<Is...>) { (qimg_write<(Is >> 3), ((Is >> 2) & 1), (Is & 3)>(qi, qx), ...); }

template <int A> DI unsigned fin2(float v0, float v1, f32x2 f1, f32x2 f2) {
  f32x2 a = {ard<A>(), ard<A + 1>()}, v = {v0, v1};
  f32x2 r = a * f1 + v * f2;
  return cvtpk(r[0], r[1]);
}
template <int KH, int F> DI void ep_fin(char* buf, const f32x4 (&pv)[16], f32x2 f1, f32x2 f2) {
  constexpr int t = F >> 1, i = F & 1, A = 16 * (4 * KH + t) + 8 * i;
  const f32x4 v = pv[4 * t + 2 * i], u = pv[4 * t + 2 * i + 1];
  u32x4 pk = {fin2<A>(v[0], v[1], f1, f2), fin2<A + 2>(v[2], v[3], f1, f2), fin2<A + 4>(u[0], u[1], f1, f2), fin2<A + 6>(u[2], u[3], f1, f2)};
  *(u32x4*)(buf + F * 1024) = pk;
}
template <int KH, int... Is> DI void ep_fin_all(char* buf, const f32x4 (&pv)[16], f32x2 f1, f32x2 f2, iseq<Is...>) { (ep_fin<KH, Is>(buf, pv, f1, f2), ...); }
template <int I> DI void out_store(char* dst) {
  constexpr int A = 8 * I;
  u32x4 pk = {cvtpk(ard<A>(), ard<A + 1>()), cvtpk(ard<A + 2>(), ard<A + 3>()), cvtpk(ard<A + 4>(), ard<A + 5>()), cvtpk(ard<A + 6>(), ard<A + 7>())};
  __builtin_nontemporal_store(pk, (u32x4*)(dst + I * 1024));
}
template <int J> DI void c_last(char* dst) {
  if constexpr (J < 7) {
    mfma_aaa<16 * J, AF + 4 * J, AQ + 4 * 14, false>();
    mfma_aaa<16 * J, AF + 4 * (J + 7), AQ + 4 * 15, false>();
  } else mfma_pad();
  GAP();
  if constexpr (J > 0) { out_store<2 * (J - 1)>(dst); out_store<2 * (J - 1) + 1>(dst); }
  GAP();
}
template <int... Js> DI void c_last_(char* dst, iseq<Js...>) { (c_last<Js>(dst), ...); }

DI unsigned ring_off(int pos) { return ((0x02134u >> (4 * pos)) & 7u) << 15; }

__global__ __launch_bounds__(256, 1) void mha_attn_kernel(const __hip_bfloat16* __restrict__ x, const __hip_bfloat16* __restrict__ kc,
                                                          const __hip_bfloat16* __restrict__ vc, const __hip_bfloat16* __restrict__ wq,
                                                          const __hip_bfloat16* __restrict__ wo, __hip_bfloat16* __restrict__ pws) {
  extern __shared__ __attribute__((aligned(16))) char lds[];
  const int tid = threadIdx.x, lane = tid & 63;
  const int w = __builtin_amdgcn_readfirstlane(tid >> 6);
  const int bid = blockIdx.x;
  const int xcd = bid & 7, idx = bid >> 3;
  const int head = xcd * 2 + (idx & 1), qh = (idx >> 1) & 1, b = idx >> 2;
  const int h = lane >> 5, r = lane & 31;

  K1Ctx c;
  c.lds = lds; c.ldsa = (unsigned)(uintptr_t)lds; c.w = w; c.lane = lane;
  c.kg = (const char*)kc + (size_t)(b * NH + head) * SK * DH * 2 + w * 8192;
  c.vg = (const char*)vc + (size_t)(b * NH + head) * SK * DH * 2 + w * 8192;
  c.wg = (const char*)wq + head * DH * 2 + (size_t)(w * 16) * (HD * 2);
  c.xg = (const char*)x + (size_t)(b * SQ + qh * 64) * (DM * 2);
  c.dstw = c.ldsa + w * 8192;
#pragma unroll
  for (int i = 0; i < 8; ++i) {
    c.kvo[i] = h * 512 + ((r ^ (2 * i + h)) << 4) + (i >> 2) * 4096;
    c.wvo[i] = (2 * i + h) * (HD * 2) + ((r ^ (((2 * i + h) & 3) << 2)) << 4) - (i & 3) * 1024;
  }
#pragma unroll
  for (int i = 0; i < 4; ++i) c.vvo[i] = h * 512 + ((r ^ (((2 * (i & 1) + h) & 3) << 2)) << 4) + (i >> 1) * 4096;
#pragma unroll
  for (int i = 0; i < 2; ++i) {
    const int row = (w * 2 + i) * 8 + (lane >> 3);
    c.xvo[i] = row * (DM * 2) + (((lane & 7) ^ ((row >> 1) & 7)) << 4) - i * 1024 + 1024;
  }
  c.xg -= 1024;
  c.ug = (const char*)wo + (size_t)head * DH * DM * 2 - 1024;
  c.dstu = c.ldsa + w * 7168;
#pragma unroll
  for (int i = 0; i < 7; ++i) {
    const int p = (7 * w + i) * 64 + lane, row = p / 56, ch = p - row * 56;
    c.uvo[i] = row * 896 + ((ch ^ (((row >> 1) & 1) << 2)) << 4) - (i & 3) * 1024 + 1024;
  }
  wstage<0>(c); wstage<1>(c); wstage<2>(c); stage_all<0>(c, c.kg, 4 * RSZ);
  {
    const int blk = (lane >> 4) & 1, qq = (lane & 15) >> 2, p = lane & 3, kh = w & 1;
    const int key = 32 * kh + r;
#pragma unroll
    for (int k = 0; k < 8; ++k) c.kaddr[k] = c.ldsa + key * 512 + (((2 * k + h) ^ (key & 15)) << 4);
#pragma unroll
    for (int t = 0; t < 4; ++t) c.vaddr[t] = c.ldsa + (32 * kh + 4 * h + qq) * 512 + 32 * blk + 8 * p + 64 * (t ^ qq);
#pragma unroll
    for (int dt = 0; dt < 2; ++dt) c.waddr[dt] = c.ldsa + (8 * h + qq) * 512 + 64 * ((2 * w + dt) ^ qq) + 32 * blk + 8 * p;
#pragma unroll
    for (int ks = 0; ks < 4; ++ks) c.xaddr[ks] = c.ldsa + XOFF + r * 128 + (((2 * ks + h) ^ ((r >> 1) & 7)) << 4);
  }
  c.pend = false; c.l = 0.f; c.alpha = 1.f; c.m = 0.f; c.ps0 = 0.f; c.ps1 = 0.f; c.pm = 0.f; c.mc = 0.f;

  wait_bar<28>(); GAP();
  wfrag0_(c, make_iseq<16>{}); GAP();
  qp_phase<1, 18>(c); qp_phase<2, 18>(c); qp_phase<3, 10>(c); qp_phase<4, 10>(c); qp_phase<5, 10>(c); qp_phase<6, 8>(c); qp_phase<7, 16>(c);
  mfma_pad();
  qimg_all(lds + 3 * RSZ, (unsigned)(r * 512 + 8 * h + (((r & 15) ^ (8 * w)) << 4)), make_iseq<16>{});
  wait_bar<32>(); GAP();
  {
    const int q = 32 * (w >> 1) + r;
    unsigned qa[8];
#pragma unroll
    for (int k = 0; k < 8; ++k) qa[k] = c.ldsa + 3 * RSZ + q * 512 + (((2 * k + h) ^ (q & 15)) << 4);
    qfrag_(qa, make_iseq<16>{});
  }
  wait_vm<32>(); GAP();

  typedef make_iseq<16> G16;
  a_phase<0, 0, 1>(c, 0, c.vg + RSZ, 4 * RSZ, true, G16{});
  mfma_pad(); wait_bar<24>(); GAP();
  b_phase<0, 0, 1>(c, 1 * RSZ, c.kg + 3 * RSZ, 3 * RSZ, true, G16{});
  wait_bar<24>(); GAP();
  a_phase<1, 1, 1>(c, 2 * RSZ, c.vg + 2 * RSZ, 1 * RSZ, true, G16{});
  mfma_pad(); wait_bar<24>(); GAP();
  b_phase<1, 2, 1>(c, 0 * RSZ, c.kg + 4 * RSZ, 2 * RSZ, true, G16{});
  int pos = 0;
  unsigned prev = 0 * RSZ;
  for (int t = 2; t < 6; t += 2) {
    unsigned r0 = ring_off(pos), r1 = ring_off(pos + 1 >= 5 ? pos - 4 : pos + 1), r2 = ring_off(pos + 2 >= 5 ? pos - 3 : pos + 2), r3 = ring_off(pos + 3 >= 5 ? pos - 2 : pos + 3);
    wait_bar<24>(); GAP();
    a_phase<0, 1, 1>(c, r0, c.vg + (size_t)(t + 1) * RSZ, prev, true, G16{});
    mfma_pad(); wait_bar<24>(); GAP();
    b_phase<0, 1, 1>(c, r1, c.kg + (size_t)(t + 3) * RSZ, r0, true, G16{});
    wait_bar<24>(); GAP();
    a_phase<1, 1, 1>(c, r2, c.vg + (size_t)(t + 2) * RSZ, r1, true, G16{});
    mfma_pad(); wait_bar<24>(); GAP();
    b_phase<1, 1, 2>(c, r3, c.kg + (size_t)(t + 4) * RSZ, r2, t + 4 < 8, G16{});
    prev = r3;
    pos = pos + 4 >= 5 ? pos - 1 : pos + 4;
  }
  wait_bar<23>(); GAP();
  a_phase<0, 1, 1>(c, 2 * RSZ, c.vg + 7 * RSZ, 1 * RSZ, true, G16{});
  mfma_pad(); wait_bar<23>(); GAP();
  b_phase<0, 1, 3>(c, 0 * RSZ, nullptr, 2 * RSZ, false, G16{});
  wait_bar<22>(); GAP();
  a_phase<1, 1, 3>(c, 4 * RSZ, nullptr, 0 * RSZ, false, G16{});
  mfma_pad(); wait_bar<22>(); GAP();
  b_phase<1, 3, 0>(c, 0, nullptr, 0, false, G16{});
  wait_bar<14>(); GAP();
  a_phase<0, 2, 0>(c, 1 * RSZ, nullptr, 0, false, G16{});
  {
    const int kh = w & 1, pw = w ^ 1;
    const float m = c.m;
    const float l = swap_half_sum(c.l);
    float* st = (float*)(lds + 3 * RSZ + 28672);
    st[w * 128 + lane] = m;
    st[w * 128 + 64 + lane] = l;
    {
      const int blk = (lane >> 4) & 1, qq = (lane & 15) >> 2, p = lane & 3;
      const unsigned base = c.ldsa + (4 * h + qq) * 896 + 32 * blk + 8 * p + kh * 448;
      const unsigned sw = (qq & 2) ? 64u : 0u;
      c.uaddr[0] = kh ? base - sw : base + sw;
      c.uaddr[1] = kh ? base + sw : base - sw;
    }
    wait_bar<14>(); GAP();
    const float m2 = st[pw * 128 + lane], l2 = st[pw * 128 + 64 + lane];
    const unsigned dst = c.ldsa + ((w & 2) ? 1 * RSZ : 4 * RSZ) + (w & 1) * 16384 + lane * 16;
    if (kh == 0) pv_last_<0>(c, dst, G16{}); else pv_last_<1>(c, dst, G16{});
    const float mn = fmaxf(m, m2);
    const float a1 = __builtin_amdgcn_exp2f((m - mn) * SM_C), a2 = __builtin_amdgcn_exp2f((m2 - mn) * SM_C);
    const float inv = 1.0f / (l * a1 + l2 * a2);
    const f32x2 f1 = {a1 * inv, a1 * inv}, f2 = {a2 * inv, a2 * inv};
    wait_bar<14>(); GAP();
    ustage_(c, 3, uslot(3) * RSZ, make_iseq<7>{});
    char* buf = lds + ((w & 2) ? 1 * RSZ : 4 * RSZ) + (pw & 1) * 16384 + lane * 16;
    f32x4 pv[16];
#pragma unroll
    for (int i = 0; i < 16; ++i) pv[i] = *(const f32x4*)(buf + i * 1024);
    if (kh == 0) ep_fin_all<0>(buf, pv, f1, f2, make_iseq<8>{}); else ep_fin_all<1>(buf, pv, f1, f2, make_iseq<8>{});
  }
  wait_bar<21>(); GAP();
  {
    const unsigned fa = c.ldsa + ((w & 2) ? 1 * RSZ : 4 * RSZ) + lane * 16;
    rd128<AQ, 16384>(fa); rd128<AQ + 4, 16384 + 1024>(fa);
    c_phase<0, 14>(c, fa); c_phase<1, 7>(c, fa); c_phase<2, 14>(c, fa); c_phase<3, 21>(c, fa); c_phase<4, 14>(c, fa); c_phase<5, 7>(c, fa); c_phase<6, 0>(c, fa);
    wait_vm<0>(); GAP();
    c_last_((char*)pws + ((size_t)((head * 16 + b * 2 + qh) * 4 + w) * 14) * 1024 + lane * 16, make_iseq<8>{});
  }
}

__global__ __launch_bounds__(256) void mha_reduce_kernel(const __hip_bfloat16* __restrict__ pws, __hip_bfloat16* __restrict__ out) {
  const int tid = threadIdx.x, blk = blockIdx.x;
  const int tile = blk / 14, rem = (blk % 14) * 256 + tid;
  const char* src = (const char*)pws + ((size_t)tile * 3584 + rem) * 16;
  u32x4 v[16];
#pragma unroll
  for (int hh = 0; hh < 16; ++hh) v[hh] = *(const u32x4*)(src + (size_t)hh * (16 * 57344));
  float s[8] = {0.f, 0.f, 0.f, 0.f, 0.f, 0.f, 0.f, 0.f};
#pragma unroll
  for (int hh = 0; hh < 16; ++hh) {
#pragma unroll
    for (int e = 0; e < 4; ++e) {
      s[2 * e] += __uint_as_float(v[hh][e] << 16);
      s[2 * e + 1] += __uint_as_float(v[hh][e] & 0xffff0000u);
    }
  }
  const int w = rem / 896, jg = (rem % 896) >> 6, lane = tid & 63;
  const int q = tile * 64 + 32 * (w >> 1) + (lane & 31);
  const int n0 = 32 * (7 * (w & 1) + (jg >> 1)) + 16 * (jg & 1) + 4 * (lane >> 5);
  char* dst = (char*)out + (size_t)q * (DM * 2) + n0 * 2;
  u32x2 lo = {cvtpk(s[0], s[1]), cvtpk(s[2], s[3])}, hi = {cvtpk(s[4], s[5]), cvtpk(s[6], s[7])};
  *(u32x2*)dst = lo;
  *(u32x2*)(dst + 16) = hi;
}

extern "C" void kernel_launch(void* const* d_in, const int* in_sizes, int n_in, void* d_out, int out_size, void* d_ws, size_t ws_size,
                              hipStream_t stream) {
  const __hip_bfloat16* x = (const __hip_bfloat16*)d_in[0];
  const __hip_bfloat16* kc = (const __hip_bfloat16*)d_in[1];
  const __hip_bfloat16* vc = (const __hip_bfloat16*)d_in[2];
  const __hip_bfloat16* wq = (const __hip_bfloat16*)d_in[3];
  const __hip_bfloat16* wo = (const __hip_bfloat16*)d_in[4];
  __hip_bfloat16* out = (__hip_bfloat16*)d_out;
  __hip_bfloat16* pws = (__hip_bfloat16*)d_ws;
  (void)hipFuncSetAttribute((const void*)mha_attn_kernel, hipFuncAttributeMaxDynamicSharedMemorySize, K1_LDS);
  mha_attn_kernel<<<256, 256, K1_LDS, stream>>>(x, kc, vc, wq, wo, pws);
  mha_reduce_kernel<<<224, 256, 0, stream>>>(pws, out);
}
```

```cpp
#include <hip/hip_runtime.h>
#include <hip/hip_bf16.h>
#include <stdint.h>
#include <utility>

typedef __attribute__((ext_vector_type(8))) short bf16x8;
typedef __attribute__((ext_vector_type(4))) short s16x4;
typedef __attribute__((ext_vector_type(16))) float f32x16;
typedef __attribute__((ext_vector_type(4))) float f32x4;
typedef __attribute__((ext_vector_type(2))) float f32x2;
typedef __attribute__((ext_vector_type(4))) unsigned u32x4;
typedef __attribute__((ext_vector_type(2))) unsigned u32x2;
typedef __attribute__((ext_vector_type(2))) __bf16 bf16x2_t;
typedef __attribute__((address_space(3))) s16x4* lds_s16x4_ptr;

#define DI __device__ __forceinline__
#define MFMA(a, b, c) __builtin_amdgcn_mfma_f32_32x32x16_bf16((a), (b), (c), 0, 0, 0)
#define GAP() __builtin_amdgcn_sched_barrier(0)
#define ACLOB "a0","a1","a2","a3","a4","a5","a6","a7","a8","a9","a10","a11","a12","a13","a14","a15","a16","a17","a18","a19","a20","a21","a22","a23","a24","a25","a26","a27","a28","a29","a30","a31","a32","a33","a34","a35","a36","a37","a38","a39","a40","a41","a42","a43","a44","a45","a46","a47","a48","a49","a50","a51","a52","a53","a54","a55","a56","a57","a58","a59","a60","a61","a62","a63","a64","a65","a66","a67","a68","a69","a70","a71","a72","a73","a74","a75","a76","a77","a78","a79","a80","a81","a82","a83","a84","a85","a86","a87","a88","a89","a90","a91","a92","a93","a94","a95","a96","a97","a98","a99","a100","a101","a102","a103","a104","a105","a106","a107","a108","a109","a110","a111","a112","a113","a114","a115","a116","a117","a118","a119","a120","a121","a122","a123","a124","a125","a126","a127","a128","a129","a130","a131","a132","a133","a134","a135","a136","a137","a138","a139","a140","a141","a142","a143","a144","a145","a146","a147","a148","a149","a150","a151","a152","a153","a154","a155","a156","a157","a158","a159","a160","a161","a162","a163","a164","a165","a166","a167","a168","a169","a170","a171","a172","a173","a174","a175","a176","a177","a178","a179","a180","a181","a182","a183","a184","a185","a186","a187","a188","a189","a190","a191","a192","a193","a194","a195","a196","a197","a198","a199","a200","a201","a202","a203","a204","a205","a206","a207","a208","a209","a210","a211","a212","a213","a214","a215","a216","a217","a218","a219","a220","a221","a222","a223","a224","a225","a226","a227","a228","a229","a230","a231","a232","a233","a234","a235","a236","a237","a238","a239","a240","a241","a242","a243","a244","a245","a246","a247","a248","a249","a250","a251","a252","a253","a254","a255"
template <int... Is> using iseq = std::integer_sequence<int, Is...>;
template <int N> using make_iseq = std::make_integer_sequence<int, N>;

constexpr int NB = 8, SQ = 128, DM = 448, NH = 16, SK = 512, DH = 256;
constexpr int HD = NH * DH;

DI unsigned cvtpk(float lo, float hi) {
  f32x2 v = {lo, hi};
  bf16x2_t r = __builtin_convertvector(v, bf16x2_t);
  return __builtin_bit_cast(unsigned, r);
}
DI void dma16(const char* sbase, unsigned voff, unsigned ldsa) {
  asm volatile("s_mov_b32 m0, %2\n\ts_nop 0\n\tglobal_load_lds_dwordx4 %0, %1" ::"v"(voff), "s"(sbase), "s"(ldsa) : "memory");
}
DI s16x4 tr_read(const char* l) {
  return __builtin_amdgcn_ds_read_tr16_b64_v4i16((lds_s16x4_ptr)(l));
}
DI bf16x8 cat8(s16x4 a, s16x4 b) { return __builtin_shufflevector(a, b, 0, 1, 2, 3, 4, 5, 6, 7); }
template <int N> DI void wait_bar() {
  asm volatile("s_waitcnt vmcnt(%0) lgkmcnt(0)\n\ts_barrier" ::"n"(N) : "memory", ACLOB);
}
template <int N> DI void wait_vm() {
  asm volatile("s_waitcnt vmcnt(%0) lgkmcnt(0)" ::"n"(N) : "memory");
}
DI float swap_half_max(float v) {
  auto rr = __builtin_amdgcn_permlane32_swap(__float_as_uint(v), __float_as_uint(v), false, false);
  return fmaxf(__uint_as_float(rr[0]), __uint_as_float(rr[1]));
}
DI float swap_half_sum(float v) {
  auto rr = __builtin_amdgcn_permlane32_swap(__float_as_uint(v), __float_as_uint(v), false, false);
  return __uint_as_float(rr[0]) + __uint_as_float(rr[1]);
}
DI float max3f(float a, float b, float c) { float r; asm("v_max3_f32 %0, %1, %2, %3" : "=v"(r) : "v"(a), "v"(b), "v"(c)); return r; }
DI float max7(float p, float a, float b, float c, float d, float e, float f) {
  float r; asm("v_max3_f32 %0, %1, %2, %3\n\tv_max3_f32 %0, %0, %4, %5\n\tv_max3_f32 %0, %0, %6, %7" : "=&v"(r) : "v"(p), "v"(a), "v"(b), "v"(c), "v"(d), "v"(e), "v"(f)); return r;
}
DI void mfma_pad() { asm volatile("s_nop 7\n\ts_nop 7" ::: "memory"); }

template <int A, int OFF> DI void rd128(unsigned addr) {
  asm volatile("ds_read_b128 a[%c1:%c2], %0 offset:%c3" ::"v"(addr), "i"(A), "i"(A + 3), "i"(OFF) : "memory");
}
template <int A, int OFF> DI void rdtr(unsigned addr) {
  asm volatile("ds_read_b64_tr_b16 a[%c1:%c2], %0 offset:%c3" ::"v"(addr), "i"(A), "i"(A + 1), "i"(OFF) : "memory");
}
template <int A, int OFF0, int OFF1> DI void rdtr2(unsigned addr) {
  asm volatile("ds_read_b64_tr_b16 a[%c1:%c2], %0 offset:%c5\n\tds_read_b64_tr_b16 a[%c3:%c4], %0 offset:%c6" ::"v"(addr), "i"(A), "i"(A + 1), "i"(A + 2), "i"(A + 3), "i"(OFF0), "i"(OFF1) : "memory");
}
template <int A, int OFF> DI void wr128a(unsigned addr) {
  asm volatile("ds_write_b128 %0, a[%c1:%c2] offset:%c3" ::"v"(addr), "i"(A), "i"(A + 3), "i"(OFF) : "memory");
}
template <int OFF> DI void dmao(const char* sbase, unsigned voff) {
  asm volatile("global_load_lds_dwordx4 %0, %1 offset:%c2" ::"v"(voff), "s"(sbase), "i"(OFF) : "memory");
}
template <int OFF> DI void dmao_m0(const char* sbase, unsigned voff, unsigned ldsa) {
  asm volatile("s_mov_b32 m0, %2\n\ts_nop 0\n\tglobal_load_lds_dwordx4 %0, %1 offset:%c3" ::"v"(voff), "s"(sbase), "s"(ldsa), "i"(OFF) : "memory");
}
template <int D, int A, int B, bool ZERO> DI void mfma_aaa() {
  if constexpr (ZERO) asm volatile("v_mfma_f32_32x32x16_bf16 a[%c0:%c1], a[%c2:%c3], a[%c4:%c5], 0" ::"i"(D), "i"(D + 15), "i"(A), "i"(A + 3), "i"(B), "i"(B + 3));
  else asm volatile("v_mfma_f32_32x32x16_bf16 a[%c0:%c1], a[%c2:%c3], a[%c4:%c5], a[%c0:%c1]" ::"i"(D), "i"(D + 15), "i"(A), "i"(A + 3), "i"(B), "i"(B + 3));
}
template <int A, int B, bool ZERO> DI void mfma_vaa(f32x16& s) {
  if constexpr (ZERO) asm volatile("v_mfma_f32_32x32x16_bf16 %0, a[%c1:%c2], a[%c3:%c4], 0" : "=v"(s) : "i"(A), "i"(A + 3), "i"(B), "i"(B + 3));
  else asm volatile("v_mfma_f32_32x32x16_bf16 %0, a[%c1:%c2], a[%c3:%c4], %0" : "+v"(s) : "i"(A), "i"(A + 3), "i"(B), "i"(B + 3));
}
template <int D, int A, bool ZERO> DI void mfma_aav(const bf16x8& p) {
  if constexpr (ZERO) asm volatile("v_mfma_f32_32x32x16_bf16 a[%c1:%c2], a[%c3:%c4], %0, 0" ::"v"(p), "i"(D), "i"(D + 15), "i"(A), "i"(A + 3));
  else asm volatile("v_mfma_f32_32x32x16_bf16 a[%c1:%c2], a[%c3:%c4], %0, a[%c1:%c2]" ::"v"(p), "i"(D), "i"(D + 15), "i"(A), "i"(A + 3));
}
template <int A> DI float ard() { float r; asm volatile("v_accvgpr_read_b32 %0, a[%c1]" : "=v"(r) : "i"(A)); return r; }
template <int A> DI void ascale1(float f) {
  float t;
  asm volatile("v_accvgpr_read_b32 %0, a[%c2]\n\tv_mul_f32 %0, %1, %0\n\tv_accvgpr_write_b32 a[%c2], %0" : "=&v"(t) : "v"(f), "i"(A) : ACLOB);
}
template <int... Is> DI void ascale_all(float f, iseq<Is...>) { (ascale1<Is>(f), ...); }

constexpr int RSZ = 32768;
constexpr int K1_LDS = 5 * RSZ;
constexpr int XOFF = 3 * RSZ;
constexpr int AQ = 128, AF = 192;
constexpr float SM_C = 0.0625f * 1.4426950408889634f;
constexpr float SM_THR = 8.0f / 0.0625f;

struct K1Ctx {
  char* lds;
  unsigned ldsa;
  const char* kg;
  const char* vg;
  const char* wg;
  const char* xg;
  int w, lane;
  unsigned kaddr[8], vaddr[4], waddr[2], xaddr[4];
  unsigned kvo[8], vvo[4], wvo[8], xvo[2];
  unsigned dstw;
  const char* ug;
  unsigned uvo[7], uaddr[2], dstu;
  f32x16 S[2];
  bf16x8 P[2], P2[2];
  float m, l, alpha, mc, ps0, ps1, pm;
  bool pend;
};

template <int TYPE, int I> DI void piece(const K1Ctx& c, const char* src, unsigned slot) {
  const unsigned vo = TYPE == 0 ? c.kvo[I] : TYPE == 1 ? c.vvo[(I & 1) + 2 * (I >> 2)] : c.wvo[I];
  if constexpr ((I & 3) == 0) dmao_m0<0>(src, vo, c.dstw + slot + (I >> 2) * 4096);
  else dmao<(I & 3) * 1024>(src, vo);
}
template <int TYPE, int... Is> DI void stage_(const K1Ctx& c, const char* src, unsigned slot, iseq<Is...>) { (piece<TYPE, Is>(c, src, slot), ...); }
template <int TYPE> DI void stage_all(const K1Ctx& c, const char* src, unsigned slot) { stage_<TYPE>(c, src, slot, make_iseq<8>{}); }
template <int S> DI void xpieces(const K1Ctx& c) {
  dmao_m0<0>(c.xg + S * 128, c.xvo[0], c.ldsa + XOFF + (S & 3) * 8192 + c.w * 2048);
  dmao<1024>(c.xg + S * 128, c.xvo[1]);
}
template <int S> DI void wstage(const K1Ctx& c) { xpieces<S>(c); stage_all<2>(c, c.wg + (size_t)(S * 64) * (HD * 2), (S % 3) * RSZ); }

template <int J> DI void kfrag(const K1Ctx& c, unsigned rd) { rd128<AF + 4 * J, 256 * (J >> 3)>(c.kaddr[J & 7] + rd); }
template <int J> DI void vfrag(const K1Ctx& c, unsigned rd) {
  constexpr int t = J >> 1, ks = J & 1;
  rdtr2<AF + 4 * J, ks * 8192 + 256 * (t >> 2), ks * 8192 + 256 * (t >> 2) + 4096>(c.vaddr[t & 3] + rd);
}
__host__ __device__ constexpr int qset(int s) { return (s & 1) ? AF : AQ; }
template <int S, int J> DI void wfrag(const K1Ctx& c) {
  if constexpr (J < 8) {
    constexpr int ks = J >> 1, dt = J & 1;
    rdtr2<qset(S) + 4 * J, ks * 8192, ks * 8192 + 2048>(c.waddr[dt] + (S % 3) * RSZ);
  } else {
    constexpr int ks = (J - 8) >> 1, qt = (J - 8) & 1;
    rd128<qset(S) + 4 * J, qt * 4096>(c.xaddr[ks] + (S & 3) * 8192);
  }
}
__host__ __device__ constexpr int worder(int g) { return (g & 3) < 2 ? 2 * (g >> 2) + (g & 1) : 8 + 2 * (g >> 2) + (g & 1); }

template <int P, int G> DI void qp_gap(K1Ctx& c) {
  constexpr int S = P - 1, ks = G >> 2, dt = (G >> 1) & 1, qt = G & 1;
  mfma_aaa<16 * (2 * dt + qt), qset(S) + 4 * (2 * ks + dt), qset(S) + 32 + 4 * (2 * ks + qt), (S == 0 && ks == 0)>();
  if constexpr (P < 7) wfrag<P, worder(G)>(c); else kfrag<G>(c, 4 * RSZ);
  if constexpr (P <= 4) {
    if constexpr (G == 0) xpieces<P + 2>(c);
    if constexpr (G >= 1 && G < 9) piece<2, G - 1>(c, c.wg + (size_t)((P + 2) * 64) * (HD * 2), ((P + 2) % 3) * RSZ);
  } else if constexpr (P == 5) { if constexpr (G < 8) piece<0, G>(c, c.kg + RSZ, 1 * RSZ); }
  else if constexpr (P == 6) { if constexpr (G < 8) piece<1, G>(c, c.vg, 2 * RSZ); }
  else { if constexpr (G < 8) piece<0, G>(c, c.kg + 2 * RSZ, 0 * RSZ); }
  GAP();
}
template <int P, int... Gs> DI void qp_phase_(K1Ctx& c, iseq<Gs...>) { (qp_gap<P, Gs>(c), ...); }
template <int P, int N> DI void qp_phase(K1Ctx& c) { wait_bar<N>(); GAP(); qp_phase_<P>(c, make_iseq<16>{}); }
template <int... Gs> DI void wfrag0_(const K1Ctx& c, iseq<Gs...>) { (wfrag<0, worder(Gs)>(c), ...); }

template <int X, int E> DI void sm_exp(K1Ctx& c) { c.S[X][E] = __builtin_amdgcn_exp2f(fmaf(c.S[X][E], SM_C, c.mc)); }
template <int X, int E> DI void sm_add(K1Ctx& c) { if constexpr (E & 1) c.ps1 += c.S[X][E]; else c.ps0 += c.S[X][E]; }
template <int X, int KS, bool LAST = false> DI void sm_pack(K1Ctx& c) {
  u32x4 w = {cvtpk(c.S[X][8 * KS + 0], c.S[X][8 * KS + 1]), cvtpk(c.S[X][8 * KS + 2], c.S[X][8 * KS + 3]),
             cvtpk(c.S[X][8 * KS + 4], c.S[X][8 * KS + 5]), cvtpk(c.S[X][8 * KS + 6], c.S[X][8 * KS + 7])};
  if constexpr (LAST) c.P2[KS] = __builtin_bit_cast(bf16x8, w); else c.P[KS] = __builtin_bit_cast(bf16x8, w);
}
template <bool FIRST> DI void sm_decide(K1Ctx& c) {
  const float pmax = swap_half_max(c.pm);
  if constexpr (FIRST) {
    c.m = pmax;
  } else {
    if (__builtin_expect(__any(pmax - c.m > SM_THR), 0)) {
      const float mn = fmaxf(c.m, pmax);
      c.alpha = __builtin_amdgcn_exp2f((c.m - mn) * SM_C);
      c.m = mn;
      c.l *= c.alpha;
      c.pend = true;
    }
  }
  c.mc = -c.m * SM_C;
  c.ps0 = 0.f; c.ps1 = 0.f;
}

template <int I> DI void upiece(const K1Ctx& c, int s, unsigned slot) {
  const char* src = c.ug + (size_t)s * 28672;
  if constexpr ((I & 3) == 0) dmao_m0<0>(src, c.uvo[I], c.dstu + slot + (I >> 2) * 4096);
  else dmao<(I & 3) * 1024>(src, c.uvo[I]);
}
template <int G> DI void ufrag(const K1Ctx& c, unsigned slot) {
  constexpr int ksl = G / 7, j = G % 7;
  rdtr2<AF + 4 * G, ksl * 14336 + 64 * j, ksl * 14336 + 64 * j + 7168>(c.uaddr[j & 1] + slot);
}
template <int... Gs> DI void ufrag_(const K1Ctx& c, unsigned slot, iseq<Gs...>) { (ufrag<Gs>(c, slot), ...); }
__host__ __device__ constexpr int uslot(int s) { return s == 0 ? 3 : s == 1 ? 2 : s == 2 ? 0 : s == 3 ? 3 : s == 4 ? 4 : s == 5 ? 1 : s == 6 ? 2 : 0; }
template <int S, int G> DI void c_gap(K1Ctx& c, unsigned fa) {
  constexpr int ksl = G / 7, j = G % 7;
  mfma_aaa<16 * j, AF + 4 * G, AQ + 4 * (2 * S + ksl), (S == 0 && ksl == 0)>();
  ufrag<G>(c, uslot(S + 1) * RSZ);
  if constexpr (S == 0) rd128<AQ + 4 * (G + 2), 1024 * ((G + 2) & 7) + (G + 2 < 8 ? 16384 : 0)>(fa);
  if constexpr (S == 1 || S == 2) { if constexpr (G < 7) upiece<G>(c, 2 * S + 2, uslot(2 * S + 2) * RSZ); else upiece<G - 7>(c, 2 * S + 3, uslot(2 * S + 3) * RSZ); }
  GAP();
}
template <int S, int... Gs> DI void c_phase_(K1Ctx& c, unsigned fa, iseq<Gs...>) { (c_gap<S, Gs>(c, fa), ...); }
template <int S, int N> DI void c_phase(K1Ctx& c, unsigned fa) { wait_bar<N>(); GAP(); c_phase_<S>(c, fa, make_iseq<14>{}); }
template <int... Is> DI void ustage_(const K1Ctx& c, int s, unsigned slot, iseq<Is...>) { (upiece<Is>(c, s, slot), ...); }

template <int X, int KIND, int DMA, int G> DI void a_gap(K1Ctx& c, unsigned rd, const char* src, unsigned dst, bool dodma) {
  constexpr int Xp = X ^ 1;
  if constexpr (KIND != 2) mfma_vaa<AF + 4 * G, AQ + 4 * G, (G == 0)>(c.S[X]);
  if constexpr (KIND != 0) {
    vfrag<G>(c, rd);
    if constexpr (G < 8) { sm_exp<Xp, 8 + G>(c); if constexpr (G > 0) sm_add<Xp, 7 + G>(c); }
    if constexpr (G == 8) { sm_add<Xp, 15>(c); sm_pack<Xp, 0>(c); }
    if constexpr (G == 9) { sm_pack<Xp, 1>(c); c.l += c.ps0 + c.ps1; }
  }
  if constexpr (DMA == 1) { if constexpr (G >= 8) piece<1, G - 8>(c, src, dst); }
  if constexpr (DMA == 3) { if constexpr (G >= 8 && G < 15) upiece<G - 8>(c, 2, dst); }
  if constexpr (DMA == 2) {
    if constexpr (G == 10) { if (dodma) { piece<1, 0>(c, src, dst); piece<1, 1>(c, src, dst); piece<1, 2>(c, src, dst); piece<1, 3>(c, src, dst); } }
    if constexpr (G == 13) { if (dodma) { piece<1, 4>(c, src, dst); piece<1, 5>(c, src, dst); piece<1, 6>(c, src, dst); piece<1, 7>(c, src, dst); } }
  }
  GAP();
}
template <int X, int KIND, int DMA, int... Gs> DI void a_phase(K1Ctx& c, unsigned rd, const char* src, unsigned dst, bool dodma, iseq<Gs...>) {
  (a_gap<X, KIND, DMA, Gs>(c, rd, src, dst, dodma), ...);
}
template <int X, int KIND, int DMA, int G> DI void b_gap(K1Ctx& c, unsigned rd, const char* src, unsigned dst, bool dodma) {
  if constexpr (KIND != 0) mfma_aav<16 * (G >> 1), AF + 4 * G, (KIND == 2 && (G & 1) == 0)>(c.P[G & 1]);
  if constexpr (KIND != 3) kfrag<G>(c, rd);
  if constexpr (G == 0) c.pm = max7(c.S[X][0], c.S[X][1], c.S[X][2], c.S[X][3], c.S[X][4], c.S[X][5], c.S[X][6]);
  if constexpr (G == 1) c.pm = max7(c.pm, c.S[X][7], c.S[X][8], c.S[X][9], c.S[X][10], c.S[X][11], c.S[X][12]);
  if constexpr (G == 2) c.pm = max7(c.pm, c.S[X][13], c.S[X][14], c.S[X][15], c.S[X][15], c.S[X][15], c.S[X][15]);
  if constexpr (G == 3) sm_decide<KIND == 0>(c);
  if constexpr (G >= 4 && G < 12) { sm_exp<X, G - 4>(c); if constexpr (G > 4) sm_add<X, G - 5>(c); }
  if constexpr (G == 12) sm_add<X, 7>(c);
  if constexpr (DMA == 1) { if constexpr (G >= 12) { piece<0, 2 * (G - 12)>(c, src, dst); piece<0, 2 * (G - 12) + 1>(c, src, dst); } }
  if constexpr (DMA == 3) { if constexpr (G >= 9) upiece<G - 9>(c, 1, dst); }
  if constexpr (DMA == 2) {
    if constexpr (G == 12) { if (dodma) { piece<0, 0>(c, src, dst); piece<0, 1>(c, src, dst); piece<0, 2>(c, src, dst); piece<0, 3>(c, src, dst); } else { upiece<0>(c, 0, dst); upiece<1>(c, 0, dst); upiece<2>(c, 0, dst); upiece<3>(c, 0, dst); } }
    if constexpr (G == 14) { if (dodma) { piece<0, 4>(c, src, dst); piece<0, 5>(c, src, dst); piece<0, 6>(c, src, dst); piece<0, 7>(c, src, dst); } else { upiece<4>(c, 0, dst); upiece<5>(c, 0, dst); upiece<6>(c, 0, dst); } }
  }
  GAP();
}
template <int X, int KIND, int DMA, int... Gs> DI void b_phase(K1Ctx& c, unsigned rd, const char* src, unsigned dst, bool dodma, iseq<Gs...>) {
  (b_gap<X, KIND, DMA, Gs>(c, rd, src, dst, dodma), ...);
  if constexpr (KIND != 0) {
    if (__builtin_expect(c.pend, 0)) { mfma_pad(); ascale_all(c.alpha, make_iseq<128>{}); asm volatile("s_nop 3"); c.pend = false; }
  }
}
template <int KH, int I> DI void ep_send(unsigned dst) {
  wr128a<16 * (4 * (1 - KH) + (I >> 2)) + 4 * (I & 3), I * 1024>(dst);
}
template <int KH, int I> DI void pv_last_gap(K1Ctx& c, unsigned dst) {
  constexpr int G = (I + 8 * (1 - KH)) & 15;
  mfma_aav<16 * (G >> 1), AF + 4 * G, false>(c.P[G & 1]);
  if constexpr (G < 14) ufrag<G>(c, uslot(0) * RSZ);
  if constexpr (I >= 8) { ep_send<KH, 2 * (I - 8)>(dst); ep_send<KH, 2 * (I - 8) + 1>(dst); }
  GAP();
}
template <int KH, int... Is> DI void pv_last_(K1Ctx& c, unsigned dst, iseq<Is...>) { (pv_last_gap<KH, Is>(c, dst), ...); }
template <int... Gs> DI void qfrag_(unsigned (&qa)[8], iseq<Gs...>) { (rd128<AQ + 4 * Gs, 256 * (Gs >> 3)>(qa[Gs & 7]), ...); }

template <int DT, int QT, int G> DI void qimg_write(char* qi, unsigned qx) {
  constexpr int A = 16 * (2 * DT + QT) + 4 * G;
  u32x2 v = {cvtpk(ard<A>(), ard<A + 1>()), cvtpk(ard<A + 2>(), ard<A + 3>())};
  *(u32x2*)(qi + QT * 32 * 512 + (qx ^ ((4 * DT + G) << 4))) = v;
}
template <int... Is> DI void qimg_all(char* qi, unsigned qx, iseq<Is...>) { (qimg_write<(Is >> 3), ((Is >> 2) & 1), (Is & 3)>(qi, qx), ...); }

template <int A> DI unsigned fin2(float v0, float v1, f32x2 f1, f32x2 f2) {
  f32x2 a = {ard<A>(), ard<A + 1>()}, v = {v0, v1};
  f32x2 r = a * f1 + v * f2;
  return cvtpk(r[0], r[1]);
}
template <int KH, int F> DI void ep_fin(char* buf, const f32x4 (&pv)[16], f32x2 f1, f32x2 f2) {
  constexpr int t = F >> 1, i = F & 1, A = 16 * (4 * KH + t) + 8 * i;
  const f32x4 v = pv[4 * t + 2 * i], u = pv[4 * t + 2 * i + 1];
  u32x4 pk = {fin2<A>(v[0], v[1], f1, f2), fin2<A + 2>(v[2], v[3], f1, f2), fin2<A + 4>(u[0], u[1], f1, f2), fin2<A + 6>(u[2], u[3], f1, f2)};
  *(u32x4*)(buf + F * 1024) = pk;
}
template <int KH, int... Is> DI void ep_fin_all(char* buf, const f32x4 (&pv)[16], f32x2 f1, f32x2 f2, iseq<Is...>) { (ep_fin<KH, Is>(buf, pv, f1, f2), ...); }
template <int I> DI void out_store(char* dst) {
  constexpr int A = 8 * I;
  u32x4 pk = {cvtpk(ard<A>(), ard<A + 1>()), cvtpk(ard<A + 2>(), ard<A + 3>()), cvtpk(ard<A + 4>(), ard<A + 5>()), cvtpk(ard<A + 6>(), ard<A + 7>())};
  __builtin_nontemporal_store(pk, (u32x4*)(dst + I * 1024));
}
template <int J> DI void c_last(char* dst) {
  if constexpr (J < 7) {
    mfma_aaa<16 * J, AF + 4 * J, AQ + 4 * 14, false>();
    mfma_aaa<16 * J, AF + 4 * (J + 7), AQ + 4 * 15, false>();
  } else mfma_pad();
  GAP();
  if constexpr (J > 0) { out_store<2 * (J - 1)>(dst); out_store<2 * (J - 1) + 1>(dst); }
  GAP();
}
template <int... Js> DI void c_last_(char* dst, iseq<Js...>) { (c_last<Js>(dst), ...); }

DI unsigned ring_off(int pos) { return ((0x02134u >> (4 * pos)) & 7u) << 15; }

__global__ __launch_bounds__(256, 1) void mha_attn_kernel(const __hip_bfloat16* __restrict__ x, const __hip_bfloat16* __restrict__ kc,
                                                          const __hip_bfloat16* __restrict__ vc, const __hip_bfloat16* __restrict__ wq,
                                                          const __hip_bfloat16* __restrict__ wo, __hip_bfloat16* __restrict__ pws) {
  extern __shared__ __attribute__((aligned(16))) char lds[];
  const int tid = threadIdx.x, lane = tid & 63;
  const int w = __builtin_amdgcn_readfirstlane(tid >> 6);
  const int bid = blockIdx.x;
  const int xcd = bid & 7, idx = bid >> 3;
  const int head = xcd * 2 + (idx & 1), qh = (idx >> 1) & 1, b = idx >> 2;
  const int h = lane >> 5, r = lane & 31;

  K1Ctx c;
  c.lds = lds; c.ldsa = (unsigned)(uintptr_t)lds; c.w = w; c.lane = lane;
  c.wg = (const char*)wq + head * DH * 2 + (size_t)(w * 16) * (HD * 2);
  c.xg = (const char*)x + (size_t)(b * SQ + qh * 64) * (DM * 2);
  c.dstw = c.ldsa + w * 8192;
#pragma unroll
  for (int i = 0; i < 8; ++i) c.wvo[i] = (2 * i + h) * (HD * 2) + ((r ^ (((2 * i + h) & 3) << 2)) << 4) - (i & 3) * 1024;
#pragma unroll
  for (int i = 0; i < 2; ++i) {
    const int row = (w * 2 + i) * 8 + (lane >> 3);
    c.xvo[i] = row * (DM * 2) + (((lane & 7) ^ ((row >> 1) & 7)) << 4) - i * 1024 + 1024;
  }
  c.xg -= 1024;
  wstage<0>(c); wstage<1>(c); wstage<2>(c);
  GAP();
  c.kg = (const char*)kc + (size_t)(b * NH + head) * SK * DH * 2 + w * 8192;
  c.vg = (const char*)vc + (size_t)(b * NH + head) * SK * DH * 2 + w * 8192;
#pragma unroll
  for (int i = 0; i < 8; ++i) c.kvo[i] = h * 512 + ((r ^ (2 * i + h)) << 4) + (i >> 2) * 4096;
  stage_all<0>(c, c.kg, 4 * RSZ);
  GAP();
#pragma unroll
  for (int i = 0; i < 4; ++i) c.vvo[i] = h * 512 + ((r ^ (((2 * (i & 1) + h) & 3) << 2)) << 4) + (i >> 1) * 4096;
  c.ug = (const char*)wo + (size_t)head * DH * DM * 2 - 1024;
  c.dstu = c.ldsa + w * 7168;
#pragma unroll
  for (int i = 0; i < 7; ++i) {
    const int p = (7 * w + i) * 64 + lane, row = p / 56, ch = p - row * 56;
    c.uvo[i] = row * 896 + ((ch ^ (((row >> 1) & 1) << 2)) << 4) - (i & 3) * 1024 + 1024;
  }
  {
    const int blk = (lane >> 4) & 1, qq = (lane & 15) >> 2, p = lane & 3, kh = w & 1;
    const int key = 32 * kh + r;
#pragma unroll
    for (int k = 0; k < 8; ++k) c.kaddr[k] = c.ldsa + key * 512 + (((2 * k + h) ^ (key & 15)) << 4);
#pragma unroll
    for (int t = 0; t < 4; ++t) c.vaddr[t] = c.ldsa + (32 * kh + 4 * h + qq) * 512 + 32 * blk + 8 * p + 64 * (t ^ qq);
#pragma unroll
    for (int dt = 0; dt < 2; ++dt) c.waddr[dt] = c.ldsa + (8 * h + qq) * 512 + 64 * ((2 * w + dt) ^ qq) + 32 * blk + 8 * p;
#pragma unroll
    for (int ks = 0; ks < 4; ++ks) c.xaddr[ks] = c.ldsa + XOFF + r * 128 + (((2 * ks + h) ^ ((r >> 1) & 7)) << 4);
  }
  c.pend = false; c.l = 0.f; c.alpha = 1.f; c.m = 0.f; c.ps0 = 0.f; c.ps1 = 0.f; c.pm = 0.f; c.mc = 0.f;

  wait_bar<28>(); GAP();
  wfrag0_(c, make_iseq<16>{}); GAP();
  qp_phase<1, 18>(c); qp_phase<2, 18>(c); qp_phase<3, 10>(c); qp_phase<4, 10>(c); qp_phase<5, 10>(c); qp_phase<6, 8>(c); qp_phase<7, 16>(c);
  mfma_pad();
  qimg_all(lds + 3 * RSZ, (unsigned)(r * 512 + 8 * h + (((r & 15) ^ (8 * w)) << 4)), make_iseq<16>{});
  wait_bar<32>(); GAP();
  {
    const int q = 32 * (w >> 1) + r;
    unsigned qa[8];
#pragma unroll
    for (int k = 0; k < 8; ++k) qa[k] = c.ldsa + 3 * RSZ + q * 512 + (((2 * k + h) ^ (q & 15)) << 4);
    qfrag_(qa, make_iseq<16>{});
  }
  wait_vm<32>(); GAP();

  typedef make_iseq<16> G16;
  a_phase<0, 0, 1>(c, 0, c.vg + RSZ, 4 * RSZ, true, G16{});
  mfma_pad(); wait_bar<24>(); GAP();
  b_phase<0, 0, 1>(c, 1 * RSZ, c.kg + 3 * RSZ, 3 * RSZ, true, G16{});
  wait_bar<24>(); GAP();
  a_phase<1, 1, 1>(c, 2 * RSZ, c.vg + 2 * RSZ, 1 * RSZ, true, G16{});
  mfma_pad(); wait_bar<24>(); GAP();
  b_phase<1, 2, 1>(c, 0 * RSZ, c.kg + 4 * RSZ, 2 * RSZ, true, G16{});
  int pos = 0;
  unsigned prev = 0 * RSZ;
  for (int t = 2; t < 6; t += 2) {
    unsigned r0 = ring_off(pos), r1 = ring_off(pos + 1 >= 5 ? pos - 4 : pos + 1), r2 = ring_off(pos + 2 >= 5 ? pos - 3 : pos + 2), r3 = ring_off(pos + 3 >= 5 ? pos - 2 : pos + 3);
    wait_bar<24>(); GAP();
    a_phase<0, 1, 1>(c, r0, c.vg + (size_t)(t + 1) * RSZ, prev, true, G16{});
    mfma_pad(); wait_bar<24>(); GAP();
    b_phase<0, 1, 1>(c, r1, c.kg + (size_t)(t + 3) * RSZ, r0, true, G16{});
    wait_bar<24>(); GAP();
    a_phase<1, 1, 1>(c, r2, c.vg + (size_t)(t + 2) * RSZ, r1, true, G16{});
    mfma_pad(); wait_bar<24>(); GAP();
    b_phase<1, 1, 2>(c, r3, c.kg + (size_t)(t + 4) * RSZ, r2, t + 4 < 8, G16{});
    prev = r3;
    pos = pos + 4 >= 5 ? pos - 1 : pos + 4;
  }
  wait_bar<23>(); GAP();
  a_phase<0, 1, 1>(c, 2 * RSZ, c.vg + 7 * RSZ, 1 * RSZ, true, G16{});
  mfma_pad(); wait_bar<23>(); GAP();
  b_phase<0, 1, 3>(c, 0 * RSZ, nullptr, 2 * RSZ, false, G16{});
  wait_bar<22>(); GAP();
  a_phase<1, 1, 3>(c, 4 * RSZ, nullptr, 0 * RSZ, false, G16{});
  mfma_pad(); wait_bar<22>(); GAP();
  b_phase<1, 3, 0>(c, 0, nullptr, 0, false, G16{});
  wait_bar<14>(); GAP();
  a_phase<0, 2, 0>(c, 1 * RSZ, nullptr, 0, false, G16{});
  {
    const int kh = w & 1, pw = w ^ 1;
    const float m = c.m;
    const float l = swap_half_sum(c.l);
    float* st = (float*)(lds + 3 * RSZ + 28672);
    st[w * 128 + lane] = m;
    st[w * 128 + 64 + lane] = l;
    {
      const int blk = (lane >> 4) & 1, qq = (lane & 15) >> 2, p = lane & 3;
      const unsigned base = c.ldsa + (4 * h + qq) * 896 + 32 * blk + 8 * p + kh * 448;
      const unsigned sw = (qq & 2) ? 64u : 0u;
      c.uaddr[0] = kh ? base - sw : base + sw;
      c.uaddr[1] = kh ? base + sw : base - sw;
    }
    wait_bar<14>(); GAP();
    const float m2 = st[pw * 128 + lane], l2 = st[pw * 128 + 64 + lane];
    const unsigned dst = c.ldsa + ((w & 2) ? 1 * RSZ : 4 * RSZ) + (w & 1) * 16384 + lane * 16;
    if (kh == 0) pv_last_<0>(c, dst, G16{}); else pv_last_<1>(c, dst, G16{});
    const float mn = fmaxf(m, m2);
    const float a1 = __builtin_amdgcn_exp2f((m - mn) * SM_C), a2 = __builtin_amdgcn_exp2f((m2 - mn) * SM_C);
    const float inv = 1.0f / (l * a1 + l2 * a2);
    const f32x2 f1 = {a1 * inv, a1 * inv}, f2 = {a2 * inv, a2 * inv};
    wait_bar<14>(); GAP();
    ustage_(c, 3, uslot(3) * RSZ, make_iseq<7>{});
    char* buf = lds + ((w & 2) ? 1 * RSZ : 4 * RSZ) + (pw & 1) * 16384 + lane * 16;
    f32x4 pv[16];
#pragma unroll
    for (int i = 0; i < 16; ++i) pv[i] = *(const f32x4*)(buf + i * 1024);
    if (kh == 0) ep_fin_all<0>(buf, pv, f1, f2, make_iseq<8>{}); else ep_fin_all<1>(buf, pv, f1, f2, make_iseq<8>{});
  }
  wait_bar<21>(); GAP();
  {
    const unsigned fa = c.ldsa + ((w & 2) ? 1 * RSZ : 4 * RSZ) + lane * 16;
    rd128<AQ, 16384>(fa); rd128<AQ + 4, 16384 + 1024>(fa);
    c_phase<0, 14>(c, fa); c_phase<1, 7>(c, fa); c_phase<2, 14>(c, fa); c_phase<3, 21>(c, fa); c_phase<4, 14>(c, fa); c_phase<5, 7>(c, fa); c_phase<6, 0>(c, fa);
    wait_vm<0>(); GAP();
    c_last_((char*)pws + ((size_t)((head * 16 + b * 2 + qh) * 4 + w) * 14) * 1024 + lane * 16, make_iseq<8>{});
  }
}

__global__ __launch_bounds__(256) void mha_reduce_kernel(const __hip_bfloat16* __restrict__ pws, __hip_bfloat16* __restrict__ out) {
  const int tid = threadIdx.x, blk = blockIdx.x;
  const int tile = blk / 14, rem = (blk % 14) * 256 + tid;
  const char* src = (const char*)pws + ((size_t)tile * 3584 + rem) * 16;
  u32x4 v[16];
#pragma unroll
  for (int hh = 0; hh < 16; ++hh) v[hh] = *(const u32x4*)(src + (size_t)hh * (16 * 57344));
  float s[8] = {0.f, 0.f, 0.f, 0.f, 0.f, 0.f, 0.f, 0.f};
#pragma unroll
  for (int hh = 0; hh < 16; ++hh) {
#pragma unroll
    for (int e = 0; e < 4; ++e) {
      s[2 * e] += __uint_as_float(v[hh][e] << 16);
      s[2 * e + 1] += __uint_as_float(v[hh][e] & 0xffff0000u);
    }
  }
  const int w = rem / 896, jg = (rem % 896) >> 6, lane = tid & 63;
  const int q = tile * 64 + 32 * (w >> 1) + (lane & 31);
  const int n0 = 32 * (7 * (w & 1) + (jg >> 1)) + 16 * (jg & 1) + 4 * (lane >> 5);
  char* dst = (char*)out + (size_t)q * (DM * 2) + n0 * 2;
  u32x2 lo = {cvtpk(s[0], s[1]), cvtpk(s[2], s[3])}, hi = {cvtpk(s[4], s[5]), cvtpk(s[6], s[7])};
  *(u32x2*)dst = lo;
  *(u32x2*)(dst + 16) = hi;
}

extern "C" void kernel_launch(void* const* d_in, const int* in_sizes, int n_in, void* d_out, int out_size, void* d_ws, size_t ws_size,
                              hipStream_t stream) {
  const __hip_bfloat16* x = (const __hip_bfloat16*)d_in[0];
  const __hip_bfloat16* kc = (const __hip_bfloat16*)d_in[1];
  const __hip_bfloat16* vc = (const __hip_bfloat16*)d_in[2];
  const __hip_bfloat16* wq = (const __hip_bfloat16*)d_in[3];
  const __hip_bfloat16* wo = (const __hip_bfloat16*)d_in[4];
  __hip_bfloat16* out = (__hip_bfloat16*)d_out;
  __hip_bfloat16* pws = (__hip_bfloat16*)d_ws;
  (void)hipFuncSetAttribute((const void*)mha_attn_kernel, hipFuncAttributeMaxDynamicSharedMemorySize, K1_LDS);
  mha_attn_kernel<<<256, 256, K1_LDS, stream>>>(x, kc, vc, wq, wo, pws);
  mha_reduce_kernel<<<224, 256, 0, stream>>>(pws, out);
}
```

```cpp
#include <hip/hip_runtime.h>
#include <hip/hip_bf16.h>
#include <stdint.h>
#include <utility>

typedef __attribute__((ext_vector_type(8))) short bf16x8;
typedef __attribute__((ext_vector_type(4))) short s16x4;
typedef __attribute__((ext_vector_type(16))) float f32x16;
typedef __attribute__((ext_vector_type(4))) float f32x4;
typedef __attribute__((ext_vector_type(2))) float f32x2;
typedef __attribute__((ext_vector_type(4))) unsigned u32x4;
typedef __attribute__((ext_vector_type(2))) unsigned u32x2;
typedef __attribute__((ext_vector_type(2))) __bf16 bf16x2_t;
typedef __attribute__((address_space(3))) s16x4* lds_s16x4_ptr;

#define DI __device__ __forceinline__
#define MFMA(a, b, c) __builtin_amdgcn_mfma_f32_32x32x16_bf16((a), (b), (c), 0, 0, 0)
#define GAP() __builtin_amdgcn_sched_barrier(0)
#define ACLOB "a0","a1","a2","a3","a4","a5","a6","a7","a8","a9","a10","a11","a12","a13","a14","a15","a16","a17","a18","a19","a20","a21","a22","a23","a24","a25","a26","a27","a28","a29","a30","a31","a32","a33","a34","a35","a36","a37","a38","a39","a40","a41","a42","a43","a44","a45","a46","a47","a48","a49","a50","a51","a52","a53","a54","a55","a56","a57","a58","a59","a60","a61","a62","a63","a64","a65","a66","a67","a68","a69","a70","a71","a72","a73","a74","a75","a76","a77","a78","a79","a80","a81","a82","a83","a84","a85","a86","a87","a88","a89","a90","a91","a92","a93","a94","a95","a96","a97","a98","a99","a100","a101","a102","a103","a104","a105","a106","a107","a108","a109","a110","a111","a112","a113","a114","a115","a116","a117","a118","a119","a120","a121","a122","a123","a124","a125","a126","a127","a128","a129","a130","a131","a132","a133","a134","a135","a136","a137","a138","a139","a140","a141","a142","a143","a144","a145","a146","a147","a148","a149","a150","a151","a152","a153","a154","a155","a156","a157","a158","a159","a160","a161","a162","a163","a164","a165","a166","a167","a168","a169","a170","a171","a172","a173","a174","a175","a176","a177","a178","a179","a180","a181","a182","a183","a184","a185","a186","a187","a188","a189","a190","a191","a192","a193","a194","a195","a196","a197","a198","a199","a200","a201","a202","a203","a204","a205","a206","a207","a208","a209","a210","a211","a212","a213","a214","a215","a216","a217","a218","a219","a220","a221","a222","a223","a224","a225","a226","a227","a228","a229","a230","a231","a232","a233","a234","a235","a236","a237","a238","a239","a240","a241","a242","a243","a244","a245","a246","a247","a248","a249","a250","a251","a252","a253","a254","a255"
template <int... Is> using iseq = std::integer_sequence<int, Is...>;
template <int N> using make_iseq = std::make_integer_sequence<int, N>;

constexpr int NB = 8, SQ = 128, DM = 448, NH = 16, SK = 512, DH = 256;
constexpr int HD = NH * DH;

DI unsigned cvtpk(float lo, float hi) {
  f32x2 v = {lo, hi};
  bf16x2_t r = __builtin_convertvector(v, bf16x2_t);
  return __builtin_bit_cast(unsigned, r);
}
DI void dma16(const char* sbase, unsigned voff, unsigned ldsa) {
  asm volatile("s_mov_b32 m0, %2\n\ts_nop 0\n\tglobal_load_lds_dwordx4 %0, %1" ::"v"(voff), "s"(sbase), "s"(ldsa) : "memory");
}
DI s16x4 tr_read(const char* l) {
  return __builtin_amdgcn_ds_read_tr16_b64_v4i16((lds_s16x4_ptr)(l));
}
DI bf16x8 cat8(s16x4 a, s16x4 b) { return __builtin_shufflevector(a, b, 0, 1, 2, 3, 4, 5, 6, 7); }
template <int N> DI void wait_bar() {
  asm volatile("s_waitcnt vmcnt(%0) lgkmcnt(0)\n\ts_barrier" ::"n"(N) : "memory");
}
template <int N> DI void wait_vm() {
  asm volatile("s_waitcnt vmcnt(%0) lgkmcnt(0)" ::"n"(N) : "memory");
}
DI float swap_half_max(float v) {
  auto rr = __builtin_amdgcn_permlane32_swap(__float_as_uint(v), __float_as_uint(v), false, false);
  return fmaxf(__uint_as_float(rr[0]), __uint_as_float(rr[1]));
}
DI float swap_half_sum(float v) {
  auto rr = __builtin_amdgcn_permlane32_swap(__float_as_uint(v), __float_as_uint(v), false, false);
  return __uint_as_float(rr[0]) + __uint_as_float(rr[1]);
}
DI float max3f(float a, float b, float c) { float r; asm("v_max3_f32 %0, %1, %2, %3" : "=v"(r) : "v"(a), "v"(b), "v"(c)); return r; }
DI float max7(float p, float a, float b, float c, float d, float e, float f) {
  float r; asm("v_max3_f32 %0, %1, %2, %3\n\tv_max3_f32 %0, %0, %4, %5\n\tv_max3_f32 %0, %0, %6, %7" : "=&v"(r) : "v"(p), "v"(a), "v"(b), "v"(c), "v"(d), "v"(e), "v"(f)); return r;
}
DI void mfma_pad() { asm volatile("s_nop 7\n\ts_nop 7" ::: "memory"); }

template <int A, int OFF> DI void rd128(unsigned addr) {
  asm volatile("ds_read_b128 a[%c1:%c2], %0 offset:%c3" ::"v"(addr), "i"(A), "i"(A + 3), "i"(OFF) : "memory", ACLOB);
}
template <int A, int OFF> DI void rdtr(unsigned addr) {
  asm volatile("ds_read_b64_tr_b16 a[%c1:%c2], %0 offset:%c3" ::"v"(addr), "i"(A), "i"(A + 1), "i"(OFF) : "memory", ACLOB);
}
template <int A, int OFF0, int OFF1> DI void rdtr2(unsigned addr) {
  asm volatile("ds_read_b64_tr_b16 a[%c1:%c2], %0 offset:%c5\n\tds_read_b64_tr_b16 a[%c3:%c4], %0 offset:%c6" ::"v"(addr), "i"(A), "i"(A + 1), "i"(A + 2), "i"(A + 3), "i"(OFF0), "i"(OFF1) : "memory", ACLOB);
}
template <int A, int OFF> DI void wr128a(unsigned addr) {
  asm volatile("ds_write_b128 %0, a[%c1:%c2] offset:%c3" ::"v"(addr), "i"(A), "i"(A + 3), "i"(OFF) : "memory");
}
template <int OFF> DI void dmao(const char* sbase, unsigned voff) {
  asm volatile("global_load_lds_dwordx4 %0, %1 offset:%c2" ::"v"(voff), "s"(sbase), "i"(OFF) : "memory");
}
template <int OFF> DI void dmao_m0(const char* sbase, unsigned voff, unsigned ldsa) {
  asm volatile("s_mov_b32 m0, %2\n\ts_nop 0\n\tglobal_load_lds_dwordx4 %0, %1 offset:%c3" ::"v"(voff), "s"(sbase), "s"(ldsa), "i"(OFF) : "memory");
}
template <int D, int A, int B, bool ZERO> DI void mfma_aaa() {
  if constexpr (ZERO) asm volatile("v_mfma_f32_32x32x16_bf16 a[%c0:%c1], a[%c2:%c3], a[%c4:%c5], 0" ::"i"(D), "i"(D + 15), "i"(A), "i"(A + 3), "i"(B), "i"(B + 3) : ACLOB);
  else asm volatile("v_mfma_f32_32x32x16_bf16 a[%c0:%c1], a[%c2:%c3], a[%c4:%c5], a[%c0:%c1]" ::"i"(D), "i"(D + 15), "i"(A), "i"(A + 3), "i"(B), "i"(B + 3) : ACLOB);
}
template <int A, int B, bool ZERO> DI void mfma_vaa(f32x16& s) {
  if constexpr (ZERO) asm volatile("v_mfma_f32_32x32x16_bf16 %0, a[%c1:%c2], a[%c3:%c4], 0" : "=v"(s) : "i"(A), "i"(A + 3), "i"(B), "i"(B + 3) : ACLOB);
  else asm volatile("v_mfma_f32_32x32x16_bf16 %0, a[%c1:%c2], a[%c3:%c4], %0" : "+v"(s) : "i"(A), "i"(A + 3), "i"(B), "i"(B + 3) : ACLOB);
}
template <int D, int A, bool ZERO> DI void mfma_aav(const bf16x8& p) {
  if constexpr (ZERO) asm volatile("v_mfma_f32_32x32x16_bf16 a[%c1:%c2], a[%c3:%c4], %0, 0" ::"v"(p), "i"(D), "i"(D + 15), "i"(A), "i"(A + 3) : ACLOB);
  else asm volatile("v_mfma_f32_32x32x16_bf16 a[%c1:%c2], a[%c3:%c4], %0, a[%c1:%c2]" ::"v"(p), "i"(D), "i"(D + 15), "i"(A), "i"(A + 3) : ACLOB);
}
template <int A> DI float ard() { float r; asm volatile("v_accvgpr_read_b32 %0, a[%c1]" : "=v"(r) : "i"(A)); return r; }
template <int A> DI void ascale1(float f) {
  float t;
  asm volatile("v_accvgpr_read_b32 %0, a[%c2]\n\tv_mul_f32 %0, %1, %0\n\tv_accvgpr_write_b32 a[%c2], %0" : "=&v"(t) : "v"(f), "i"(A) : ACLOB);
}
template <int... Is> DI void ascale_all(float f, iseq<Is...>) { (ascale1<Is>(f), ...); }

constexpr int RSZ = 32768;
constexpr int K1_LDS = 5 * RSZ;
constexpr int XOFF = 3 * RSZ;
constexpr int AQ = 128, AF = 192;
constexpr float SM_C = 0.0625f * 1.4426950408889634f;
constexpr float SM_THR = 8.0f / 0.0625f;

struct K1Ctx {
  char* lds;
  unsigned ldsa;
  const char* kg;
  const char* vg;
  const char* wg;
  const char* xg;
  int w, lane;
  unsigned kaddr[8], vaddr[4], waddr[2], xaddr[4];
  unsigned kvo[8], vvo[4], wvo[8], xvo[2];
  unsigned dstw;
  const char* ug;
  unsigned uvo[7], uaddr[2], dstu;
  f32x16 S[2];
  bf16x8 P[2], P2[2];
  float m, l, alpha, mc, ps0, ps1, pm;
  bool pend;
};

template <int TYPE, int I> DI void piece(const K1Ctx& c, const char* src, unsigned slot) {
  const unsigned vo = TYPE == 0 ? c.kvo[I] : TYPE == 1 ? c.vvo[(I & 1) + 2 * (I >> 2)] : c.wvo[I];
  if constexpr ((I & 3) == 0) dmao_m0<0>(src, vo, c.dstw + slot + (I >> 2) * 4096);
  else dmao<(I & 3) * 1024>(src, vo);
}
template <int TYPE, int... Is> DI void stage_(const K1Ctx& c, const char* src, unsigned slot, iseq<Is...>) { (piece<TYPE, Is>(c, src, slot), ...); }
template <int TYPE> DI void stage_all(const K1Ctx& c, const char* src, unsigned slot) { stage_<TYPE>(c, src, slot, make_iseq<8>{}); }
template <int S> DI void xpieces(const K1Ctx& c) {
  dmao_m0<0>(c.xg + S * 128, c.xvo[0], c.ldsa + XOFF + (S & 3) * 8192 + c.w * 2048);
  dmao<1024>(c.xg + S * 128, c.xvo[1]);
}
template <int S> DI void wstage(const K1Ctx& c) { xpieces<S>(c); stage_all<2>(c, c.wg + (size_t)(S * 64) * (HD * 2), (S % 3) * RSZ); }

template <int J> DI void kfrag(const K1Ctx& c, unsigned rd) { rd128<AF + 4 * J, 256 * (J >> 3)>(c.kaddr[J & 7] + rd); }
template <int J> DI void vfrag(const K1Ctx& c, unsigned rd) {
  constexpr int t = J >> 1, ks = J & 1;
  rdtr2<AF + 4 * J, ks * 8192 + 256 * (t >> 2), ks * 8192 + 256 * (t >> 2) + 4096>(c.vaddr[t & 3] + rd);
}
__host__ __device__ constexpr int qset(int s) { return (s & 1) ? AF : AQ; }
template <int S, int J> DI void wfrag(const K1Ctx& c) {
  if constexpr (J < 8) {
    constexpr int ks = J >> 1, dt = J & 1;
    rdtr2<qset(S) + 4 * J, ks * 8192, ks * 8192 + 2048>(c.waddr[dt] + (S % 3) * RSZ);
  } else {
    constexpr int ks = (J - 8) >> 1, qt = (J - 8) & 1;
    rd128<qset(S) + 4 * J, qt * 4096>(c.xaddr[ks] + (S & 3) * 8192);
  }
}
__host__ __device__ constexpr int worder(int g) { return (g & 3) < 2 ? 2 * (g >> 2) + (g & 1) : 8 + 2 * (g >> 2) + (g & 1); }

template <int P, int G> DI void qp_gap(K1Ctx& c) {
  constexpr int S = P - 1, ks = G >> 2, dt = (G >> 1) & 1, qt = G & 1;
  mfma_aaa<16 * (2 * dt + qt), qset(S) + 4 * (2 * ks + dt), qset(S) + 32 + 4 * (2 * ks + qt), (S == 0 && ks == 0)>();
  if constexpr (P < 7) wfrag<P, worder(G)>(c); else kfrag<G>(c, 4 * RSZ);
  if constexpr (P <= 4) {
    if constexpr (G == 0) xpieces<P + 2>(c);
    if constexpr (G >= 1 && G < 9) piece<2, G - 1>(c, c.wg + (size_t)((P + 2) * 64) * (HD * 2), ((P + 2) % 3) * RSZ);
  } else if constexpr (P == 5) { if constexpr (G < 8) piece<0, G>(c, c.kg + RSZ, 1 * RSZ); }
  else if constexpr (P == 6) { if constexpr (G < 8) piece<1, G>(c, c.vg, 2 * RSZ); }
  else { if constexpr (G < 8) piece<0, G>(c, c.kg + 2 * RSZ, 0 * RSZ); }
  GAP();
}
template <int P, int... Gs> DI void qp_phase_(K1Ctx& c, iseq<Gs...>) { (qp_gap<P, Gs>(c), ...); }
template <int P, int N> DI void qp_phase(K1Ctx& c) { wait_bar<N>(); GAP(); qp_phase_<P>(c, make_iseq<16>{}); }
template <int... Gs> DI void wfrag0_(const K1Ctx& c, iseq<Gs...>) { (wfrag<0, worder(Gs)>(c), ...); }

template <int X, int E> DI void sm_exp(K1Ctx& c) { c.S[X][E] = __builtin_amdgcn_exp2f(fmaf(c.S[X][E], SM_C, c.mc)); }
template <int X, int E> DI void sm_add(K1Ctx& c) { if constexpr (E & 1) c.ps1 += c.S[X][E]; else c.ps0 += c.S[X][E]; }
template <int X, int KS, bool LAST = false> DI void sm_pack(K1Ctx& c) {
  u32x4 w = {cvtpk(c.S[X][8 * KS + 0], c.S[X][8 * KS + 1]), cvtpk(c.S[X][8 * KS + 2], c.S[X][8 * KS + 3]),
             cvtpk(c.S[X][8 * KS + 4], c.S[X][8 * KS + 5]), cvtpk(c.S[X][8 * KS + 6], c.S[X][8 * KS + 7])};
  if constexpr (LAST) c.P2[KS] = __builtin_bit_cast(bf16x8, w); else c.P[KS] = __builtin_bit_cast(bf16x8, w);
}
template <bool FIRST> DI void sm_decide(K1Ctx& c) {
  const float pmax = swap_half_max(c.pm);
  if constexpr (FIRST) {
    c.m = pmax;
  } else {
    if (__builtin_expect(__any(pmax - c.m > SM_THR), 0)) {
      const float mn = fmaxf(c.m, pmax);
      c.alpha = __builtin_amdgcn_exp2f((c.m - mn) * SM_C);
      c.m = mn;
      c.l *= c.alpha;
      c.pend = true;
    }
  }
  c.mc = -c.m * SM_C;
  c.ps0 = 0.f; c.ps1 = 0.f;
}

template <int I> DI void upiece(const K1Ctx& c, int s, unsigned slot) {
  const char* src = c.ug + (size_t)s * 28672;
  if constexpr ((I & 3) == 0) dmao_m0<0>(src, c.uvo[I], c.dstu + slot + (I >> 2) * 4096);
  else dmao<(I & 3) * 1024>(src, c.uvo[I]);
}
template <int G> DI void ufrag(const K1Ctx& c, unsigned slot) {
  constexpr int ksl = G / 7, j = G % 7;
  rdtr2<AF + 4 * G, ksl * 14336 + 64 * j, ksl * 14336 + 64 * j + 7168>(c.uaddr[j & 1] + slot);
}
template <int... Gs> DI void ufrag_(const K1Ctx& c, unsigned slot, iseq<Gs...>) { (ufrag<Gs>(c, slot), ...); }
__host__ __device__ constexpr int uslot(int s) { return s == 0 ? 3 : s == 1 ? 2 : s == 2 ? 0 : s == 3 ? 3 : s == 4 ? 4 : s == 5 ? 1 : s == 6 ? 2 : 0; }
template <int S, int G> DI void c_gap(K1Ctx& c, unsigned fa) {
  constexpr int ksl = G / 7, j = G % 7;
  mfma_aaa<16 * j, AF + 4 * G, AQ + 4 * (2 * S + ksl), (S == 0 && ksl == 0)>();
  ufrag<G>(c, uslot(S + 1) * RSZ);
  if constexpr (S == 0) rd128<AQ + 4 * (G + 2), 1024 * ((G + 2) & 7) + (G + 2 < 8 ? 16384 : 0)>(fa);
  if constexpr (S == 1 || S == 2) { if constexpr (G < 7) upiece<G>(c, 2 * S + 2, uslot(2 * S + 2) * RSZ); else upiece<G - 7>(c, 2 * S + 3, uslot(2 * S + 3) * RSZ); }
  GAP();
}
template <int S, int... Gs> DI void c_phase_(K1Ctx& c, unsigned fa, iseq<Gs...>) { (c_gap<S, Gs>(c, fa), ...); }
template <int S, int N> DI void c_phase(K1Ctx& c, unsigned fa) { wait_bar<N>(); GAP(); c_phase_<S>(c, fa, make_iseq<14>{}); }
template <int... Is> DI void ustage_(const K1Ctx& c, int s, unsigned slot, iseq<Is...>) { (upiece<Is>(c, s, slot), ...); }

template <int X, int KIND, int DMA, int G> DI void a_gap(K1Ctx& c, unsigned rd, const char* src, unsigned dst, bool dodma) {
  constexpr int Xp = X ^ 1;
  if constexpr (KIND != 2) mfma_vaa<AF + 4 * G, AQ + 4 * G, (G == 0)>(c.S[X]);
  if constexpr (KIND != 0) {
    vfrag<G>(c, rd);
    if constexpr (G < 8) { sm_exp<Xp, 8 + G>(c); if constexpr (G > 0) sm_add<Xp, 7 + G>(c); }
    if constexpr (G == 8) { sm_add<Xp, 15>(c); sm_pack<Xp, 0>(c); }
    if constexpr (G == 9) { sm_pack<Xp, 1>(c); c.l += c.ps0 + c.ps1; }
  }
  if constexpr (DMA == 1) { if constexpr (G >= 8) piece<1, G - 8>(c, src, dst); }
  if constexpr (DMA == 3) { if constexpr (G >= 8 && G < 15) upiece<G - 8>(c, 2, dst); }
  if constexpr (DMA == 2) {
    if constexpr (G == 10) { if (dodma) { piece<1, 0>(c, src, dst); piece<1, 1>(c, src, dst); piece<1, 2>(c, src, dst); piece<1, 3>(c, src, dst); } }
    if constexpr (G == 13) { if (dodma) { piece<1, 4>(c, src, dst); piece<1, 5>(c, src, dst); piece<1, 6>(c, src, dst); piece<1, 7>(c, src, dst); } }
  }
  GAP();
}
template <int X, int KIND, int DMA, int... Gs> DI void a_phase(K1Ctx& c, unsigned rd, const char* src, unsigned dst, bool dodma, iseq<Gs...>) {
  (a_gap<X, KIND, DMA, Gs>(c, rd, src, dst, dodma), ...);
}
template <int X, int KIND, int DMA, int G> DI void b_gap(K1Ctx& c, unsigned rd, const char* src, unsigned dst, bool dodma) {
  if constexpr (KIND != 0) mfma_aav<16 * (G >> 1), AF + 4 * G, (KIND == 2 && (G & 1) == 0)>(c.P[G & 1]);
  if constexpr (KIND != 3) kfrag<G>(c, rd);
  if constexpr (G == 0) c.pm = max7(c.S[X][0], c.S[X][1], c.S[X][2], c.S[X][3], c.S[X][4], c.S[X][5], c.S[X][6]);
  if constexpr (G == 1) c.pm = max7(c.pm, c.S[X][7], c.S[X][8], c.S[X][9], c.S[X][10], c.S[X][11], c.S[X][12]);
  if constexpr (G == 2) c.pm = max7(c.pm, c.S[X][13], c.S[X][14], c.S[X][15], c.S[X][15], c.S[X][15], c.S[X][15]);
  if constexpr (G == 3) sm_decide<KIND == 0>(c);
  if constexpr (G >= 4 && G < 12) { sm_exp<X, G - 4>(c); if constexpr (G > 4) sm_add<X, G - 5>(c); }
  if constexpr (G == 12) sm_add<X, 7>(c);
  if constexpr (DMA == 1) { if constexpr (G >= 12) { piece<0, 2 * (G - 12)>(c, src, dst); piece<0, 2 * (G - 12) + 1>(c, src, dst); } }
  if constexpr (DMA == 3) { if constexpr (G >= 9) upiece<G - 9>(c, 1, dst); }
  if constexpr (DMA == 2) {
    if constexpr (G == 12) { if (dodma) { piece<0, 0>(c, src, dst); piece<0, 1>(c, src, dst); piece<0, 2>(c, src, dst); piece<0, 3>(c, src, dst); } else { upiece<0>(c, 0, dst); upiece<1>(c, 0, dst); upiece<2>(c, 0, dst); upiece<3>(c, 0, dst); } }
    if constexpr (G == 14) { if (dodma) { piece<0, 4>(c, src, dst); piece<0, 5>(c, src, dst); piece<0, 6>(c, src, dst); piece<0, 7>(c, src, dst); } else { upiece<4>(c, 0, dst); upiece<5>(c, 0, dst); upiece<6>(c, 0, dst); } }
  }
  GAP();
}
template <int X, int KIND, int DMA, int... Gs> DI void b_phase(K1Ctx& c, unsigned rd, const char* src, unsigned dst, bool dodma, iseq<Gs...>) {
  (b_gap<X, KIND, DMA, Gs>(c, rd, src, dst, dodma), ...);
  if constexpr (KIND != 0) {
    if (__builtin_expect(c.pend, 0)) { mfma_pad(); ascale_all(c.alpha, make_iseq<128>{}); asm volatile("s_nop 3"); c.pend = false; }
  }
}
template <int KH, int I> DI void ep_send(unsigned dst) {
  wr128a<16 * (4 * (1 - KH) + (I >> 2)) + 4 * (I & 3), I * 1024>(dst);
}
template <int KH, int I> DI void pv_last_gap(K1Ctx& c, unsigned dst) {
  constexpr int G = (I + 8 * (1 - KH)) & 15;
  mfma_aav<16 * (G >> 1), AF + 4 * G, false>(c.P[G & 1]);
  if constexpr (G < 14) ufrag<G>(c, uslot(0) * RSZ);
  if constexpr (I >= 8) { ep_send<KH, 2 * (I - 8)>(dst); ep_send<KH, 2 * (I - 8) + 1>(dst); }
  GAP();
}
template <int KH, int... Is> DI void pv_last_(K1Ctx& c, unsigned dst, iseq<Is...>) { (pv_last_gap<KH, Is>(c, dst), ...); }
template <int... Gs> DI void qfrag_(unsigned (&qa)[8], iseq<Gs...>) { (rd128<AQ + 4 * Gs, 256 * (Gs >> 3)>(qa[Gs & 7]), ...); }

template <int DT, int QT, int G> DI void qimg_write(char* qi, unsigned qx) {
  constexpr int A = 16 * (2 * DT + QT) + 4 * G;
  u32x2 v = {cvtpk(ard<A>(), ard<A + 1>()), cvtpk(ard<A + 2>(), ard<A + 3>())};
  *(u32x2*)(qi + QT * 32 * 512 + (qx ^ ((4 * DT + G) << 4))) = v;
}
template <int... Is> DI void qimg_all(char* qi, unsigned qx, iseq<Is...>) { (qimg_write<(Is >> 3), ((Is >> 2) & 1), (Is & 3)>(qi, qx), ...); }

template <int A> DI unsigned fin2(float v0, float v1, f32x2 f1, f32x2 f2) {
  f32x2 a = {ard<A>(), ard<A + 1>()}, v = {v0, v1};
  f32x2 r = a * f1 + v * f2;
  return cvtpk(r[0], r[1]);
}
template <int KH, int F> DI void ep_fin(char* buf, const f32x4 (&pv)[16], f32x2 f1, f32x2 f2) {
  constexpr int t = F >> 1, i = F & 1, A = 16 * (4 * KH + t) + 8 * i;
  const f32x4 v = pv[4 * t + 2 * i], u = pv[4 * t + 2 * i + 1];
  u32x4 pk = {fin2<A>(v[0], v[1], f1, f2), fin2<A + 2>(v[2], v[3], f1, f2), fin2<A + 4>(u[0], u[1], f1, f2), fin2<A + 6>(u[2], u[3], f1, f2)};
  *(u32x4*)(buf + F * 1024) = pk;
}
template <int KH, int... Is> DI void ep_fin_all(char* buf, const f32x4 (&pv)[16], f32x2 f1, f32x2 f2, iseq<Is...>) { (ep_fin<KH, Is>(buf, pv, f1, f2), ...); }
template <int I> DI void out_store(char* dst) {
  constexpr int A = 8 * I;
  u32x4 pk = {cvtpk(ard<A>(), ard<A + 1>()), cvtpk(ard<A + 2>(), ard<A + 3>()), cvtpk(ard<A + 4>(), ard<A + 5>()), cvtpk(ard<A + 6>(), ard<A + 7>())};
  __builtin_nontemporal_store(pk, (u32x4*)(dst + I * 1024));
}
template <int J> DI void c_last(char* dst) {
  if constexpr (J < 7) {
    mfma_aaa<16 * J, AF + 4 * J, AQ + 4 * 14, false>();
    mfma_aaa<16 * J, AF + 4 * (J + 7), AQ + 4 * 15, false>();
  } else mfma_pad();
  GAP();
  if constexpr (J > 0) { out_store<2 * (J - 1)>(dst); out_store<2 * (J - 1) + 1>(dst); }
  GAP();
}
template <int... Js> DI void c_last_(char* dst, iseq<Js...>) { (c_last<Js>(dst), ...); }

DI unsigned ring_off(int pos) { return ((0x02134u >> (4 * pos)) & 7u) << 15; }

__global__ __launch_bounds__(256, 1) void mha_attn_kernel(const __hip_bfloat16* __restrict__ x, const __hip_bfloat16* __restrict__ kc,
                                                          const __hip_bfloat16* __restrict__ vc, const __hip_bfloat16* __restrict__ wq,
                                                          const __hip_bfloat16* __restrict__ wo, __hip_bfloat16* __restrict__ pws) {
  extern __shared__ __attribute__((aligned(16))) char lds[];
  const int tid = threadIdx.x, lane = tid & 63;
  const int w = __builtin_amdgcn_readfirstlane(tid >> 6);
  const int bid = blockIdx.x;
  const int xcd = bid & 7, idx = bid >> 3;
  const int head = xcd * 2 + (idx & 1), qh = (idx >> 1) & 1, b = idx >> 2;
  const int h = lane >> 5, r = lane & 31;

  K1Ctx c;
  c.lds = lds; c.ldsa = (unsigned)(uintptr_t)lds; c.w = w; c.lane = lane;
  c.wg = (const char*)wq + head * DH * 2 + (size_t)(w * 16) * (HD * 2);
  c.xg = (const char*)x + (size_t)(b * SQ + qh * 64) * (DM * 2);
  c.dstw = c.ldsa + w * 8192;
#pragma unroll
  for (int i = 0; i < 8; ++i) c.wvo[i] = (2 * i + h) * (HD * 2) + ((r ^ (((2 * i + h) & 3) << 2)) << 4) - (i & 3) * 1024;
#pragma unroll
  for (int i = 0; i < 2; ++i) {
    const int row = (w * 2 + i) * 8 + (lane >> 3);
    c.xvo[i] = row * (DM * 2) + (((lane & 7) ^ ((row >> 1) & 7)) << 4) - i * 1024 + 1024;
  }
  c.xg -= 1024;
  wstage<0>(c); wstage<1>(c); wstage<2>(c);
  GAP();
  c.kg = (const char*)kc + (size_t)(b * NH + head) * SK * DH * 2 + w * 8192;
  c.vg = (const char*)vc + (size_t)(b * NH + head) * SK * DH * 2 + w * 8192;
#pragma unroll
  for (int i = 0; i < 8; ++i) c.kvo[i] = h * 512 + ((r ^ (2 * i + h)) << 4) + (i >> 2) * 4096;
  stage_all<0>(c, c.kg, 4 * RSZ);
  GAP();
#pragma unroll
  for (int i = 0; i < 4; ++i) c.vvo[i] = h * 512 + ((r ^ (((2 * (i & 1) + h) & 3) << 2)) << 4) + (i >> 1) * 4096;
  c.ug = (const char*)wo + (size_t)head * DH * DM * 2 - 1024;
  c.dstu = c.ldsa + w * 7168;
#pragma unroll
  for (int i = 0; i < 7; ++i) {
    const int p = (7 * w + i) * 64 + lane, row = p / 56, ch = p - row * 56;
    c.uvo[i] = row * 896 + ((ch ^ (((row >> 1) & 1) << 2)) << 4) - (i & 3) * 1024 + 1024;
  }
  {
    const int blk = (lane >> 4) & 1, qq = (lane & 15) >> 2, p = lane & 3, kh = w & 1;
    const int key = 32 * kh + r;
#pragma unroll
    for (int k = 0; k < 8; ++k) c.kaddr[k] = c.ldsa + key * 512 + (((2 * k + h) ^ (key & 15)) << 4);
#pragma unroll
    for (int t = 0; t < 4; ++t) c.vaddr[t] = c.ldsa + (32 * kh + 4 * h + qq) * 512 + 32 * blk + 8 * p + 64 * (t ^ qq);
#pragma unroll
    for (int dt = 0; dt < 2; ++dt) c.waddr[dt] = c.ldsa + (8 * h + qq) * 512 + 64 * ((2 * w + dt) ^ qq) + 32 * blk + 8 * p;
#pragma unroll
    for (int ks = 0; ks < 4; ++ks) c.xaddr[ks] = c.ldsa + XOFF + r * 128 + (((2 * ks + h) ^ ((r >> 1) & 7)) << 4);
  }
  c.pend = false; c.l = 0.f; c.alpha = 1.f; c.m = 0.f; c.ps0 = 0.f; c.ps1 = 0.f; c.pm = 0.f; c.mc = 0.f;

  wait_bar<28>(); GAP();
  wfrag0_(c, make_iseq<16>{}); GAP();
  qp_phase<1, 18>(c); qp_phase<2, 18>(c); qp_phase<3, 10>(c); qp_phase<4, 10>(c); qp_phase<5, 10>(c); qp_phase<6, 8>(c); qp_phase<7, 16>(c);
  mfma_pad();
  qimg_all(lds + 3 * RSZ, (unsigned)(r * 512 + 8 * h + (((r & 15) ^ (8 * w)) << 4)), make_iseq<16>{});
  wait_bar<32>(); GAP();
  {
    const int q = 32 * (w >> 1) + r;
    unsigned qa[8];
#pragma unroll
    for (int k = 0; k < 8; ++k) qa[k] = c.ldsa + 3 * RSZ + q * 512 + (((2 * k + h) ^ (q & 15)) << 4);
    qfrag_(qa, make_iseq<16>{});
  }
  wait_vm<32>(); GAP();

  typedef make_iseq<16> G16;
  a_phase<0, 0, 1>(c, 0, c.vg + RSZ, 4 * RSZ, true, G16{});
  mfma_pad(); wait_bar<24>(); GAP();
  b_phase<0, 0, 1>(c, 1 * RSZ, c.kg + 3 * RSZ, 3 * RSZ, true, G16{});
  wait_bar<24>(); GAP();
  a_phase<1, 1, 1>(c, 2 * RSZ, c.vg + 2 * RSZ, 1 * RSZ, true, G16{});
  mfma_pad(); wait_bar<24>(); GAP();
  b_phase<1, 2, 1>(c, 0 * RSZ, c.kg + 4 * RSZ, 2 * RSZ, true, G16{});
  int pos = 0;
  unsigned prev = 0 * RSZ;
  for (int t = 2; t < 6; t += 2) {
    unsigned r0 = ring_off(pos), r1 = ring_off(pos + 1 >= 5 ? pos - 4 : pos + 1), r2 = ring_off(pos + 2 >= 5 ? pos - 3 : pos + 2), r3 = ring_off(pos + 3 >= 5 ? pos - 2 : pos + 3);
    wait_bar<24>(); GAP();
    a_phase<0, 1, 1>(c, r0, c.vg + (size_t)(t + 1) * RSZ, prev, true, G16{});
    mfma_pad(); wait_bar<24>(); GAP();
    b_phase<0, 1, 1>(c, r1, c.kg + (size_t)(t + 3) * RSZ, r0, true, G16{});
    wait_bar<24>(); GAP();
    a_phase<1, 1, 1>(c, r2, c.vg + (size_t)(t + 2) * RSZ, r1, true, G16{});
    mfma_pad(); wait_bar<24>(); GAP();
    b_phase<1, 1, 2>(c, r3, c.kg + (size_t)(t + 4) * RSZ, r2, t + 4 < 8, G16{});
    prev = r3;
    pos = pos + 4 >= 5 ? pos - 1 : pos + 4;
  }
  wait_bar<23>(); GAP();
  a_phase<0, 1, 1>(c, 2 * RSZ, c.vg + 7 * RSZ, 1 * RSZ, true, G16{});
  mfma_pad(); wait_bar<23>(); GAP();
  b_phase<0, 1, 3>(c, 0 * RSZ, nullptr, 2 * RSZ, false, G16{});
  wait_bar<22>(); GAP();
  a_phase<1, 1, 3>(c, 4 * RSZ, nullptr, 0 * RSZ, false, G16{});
  mfma_pad(); wait_bar<22>(); GAP();
  b_phase<1, 3, 0>(c, 0, nullptr, 0, false, G16{});
  wait_bar<14>(); GAP();
  a_phase<0, 2, 0>(c, 1 * RSZ, nullptr, 0, false, G16{});
  {
    const int kh = w & 1, pw = w ^ 1;
    const float m = c.m;
    const float l = swap_half_sum(c.l);
    float* st = (float*)(lds + 3 * RSZ + 28672);
    st[w * 128 + lane] = m;
    st[w * 128 + 64 + lane] = l;
    {
      const int blk = (lane >> 4) & 1, qq = (lane & 15) >> 2, p = lane & 3;
      const unsigned base = c.ldsa + (4 * h + qq) * 896 + 32 * blk + 8 * p + kh * 448;
      const unsigned sw = (qq & 2) ? 64u : 0u;
      c.uaddr[0] = kh ? base - sw : base + sw;
      c.uaddr[1] = kh ? base + sw : base - sw;
    }
    wait_bar<14>(); GAP();
    const float m2 = st[pw * 128 + lane], l2 = st[pw * 128 + 64 + lane];
    const unsigned dst = c.ldsa + ((w & 2) ? 1 * RSZ : 4 * RSZ) + (w & 1) * 16384 + lane * 16;
    if (kh == 0) pv_last_<0>(c, dst, G16{}); else pv_last_<1>(c, dst, G16{});
    const float mn = fmaxf(m, m2);
    const float a1 = __builtin_amdgcn_exp2f((m - mn) * SM_C), a2 = __builtin_amdgcn_exp2f((m2 - mn) * SM_C);
    const float inv = 1.0f / (l * a1 + l2 * a2);
    const f32x2 f1 = {a1 * inv, a1 * inv}, f2 = {a2 * inv, a2 * inv};
    wait_bar<14>(); GAP();
    ustage_(c, 3, uslot(3) * RSZ, make_iseq<7>{});
    char* buf = lds + ((w & 2) ? 1 * RSZ : 4 * RSZ) + (pw & 1) * 16384 + lane * 16;
    f32x4 pv[16];
#pragma unroll
    for (int i = 0; i < 16; ++i) pv[i] = *(const f32x4*)(buf + i * 1024);
    if (kh == 0) ep_fin_all<0>(buf, pv, f1, f2, make_iseq<8>{}); else ep_fin_all<1>(buf, pv, f1, f2, make_iseq<8>{});
  }
  wait_bar<21>(); GAP();
  {
    const unsigned fa = c.ldsa + ((w & 2) ? 1 * RSZ : 4 * RSZ) + lane * 16;
    rd128<AQ, 16384>(fa); rd128<AQ + 4, 16384 + 1024>(fa);
    c_phase<0, 14>(c, fa); c_phase<1, 7>(c, fa); c_phase<2, 14>(c, fa); c_phase<3, 21>(c, fa); c_phase<4, 14>(c, fa); c_phase<5, 7>(c, fa); c_phase<6, 0>(c, fa);
    wait_vm<0>(); GAP();
    c_last_((char*)pws + ((size_t)((head * 16 + b * 2 + qh) * 4 + w) * 14) * 1024 + lane * 16, make_iseq<8>{});
  }
}

__global__ __launch_bounds__(256) void mha_reduce_kernel(const __hip_bfloat16* __restrict__ pws, __hip_bfloat16* __restrict__ out) {
  const int tid = threadIdx.x, blk = blockIdx.x;
  const int tile = blk / 14, rem = (blk % 14) * 256 + tid;
  const char* src = (const char*)pws + ((size_t)tile * 3584 + rem) * 16;
  u32x4 v[16];
#pragma unroll
  for (int hh = 0; hh < 16; ++hh) v[hh] = *(const u32x4*)(src + (size_t)hh * (16 * 57344));
  float s[8] = {0.f, 0.f, 0.f, 0.f, 0.f, 0.f, 0.f, 0.f};
#pragma unroll
  for (int hh = 0; hh < 16; ++hh) {
#pragma unroll
    for (int e = 0; e < 4; ++e) {
      s[2 * e] += __uint_as_float(v[hh][e] << 16);
      s[2 * e + 1] += __uint_as_float(v[hh][e] & 0xffff0000u);
    }
  }
  const int w = rem / 896, jg = (rem % 896) >> 6, lane = tid & 63;
  const int q = tile * 64 + 32 * (w >> 1) + (lane & 31);
  const int n0 = 32 * (7 * (w & 1) + (jg >> 1)) + 16 * (jg & 1) + 4 * (lane >> 5);
  char* dst = (char*)out + (size_t)q * (DM * 2) + n0 * 2;
  u32x2 lo = {cvtpk(s[0], s[1]), cvtpk(s[2], s[3])}, hi = {cvtpk(s[4], s[5]), cvtpk(s[6], s[7])};
  *(u32x2*)dst = lo;
  *(u32x2*)(dst + 16) = hi;
}

extern "C" void kernel_launch(void* const* d_in, const int* in_sizes, int n_in, void* d_out, int out_size, void* d_ws, size_t ws_size,
                              hipStream_t stream) {
  const __hip_bfloat16* x = (const __hip_bfloat16*)d_in[0];
  const __hip_bfloat16* kc = (const __hip_bfloat16*)d_in[1];
  const __hip_bfloat16* vc = (const __hip_bfloat16*)d_in[2];
  const __hip_bfloat16* wq = (const __hip_bfloat16*)d_in[3];
  const __hip_bfloat16* wo = (const __hip_bfloat16*)d_in[4];
  __hip_bfloat16* out = (__hip_bfloat16*)d_out;
  __hip_bfloat16* pws = (__hip_bfloat16*)d_ws;
  (void)hipFuncSetAttribute((const void*)mha_attn_kernel, hipFuncAttributeMaxDynamicSharedMemorySize, K1_LDS);
  mha_attn_kernel<<<256, 256, K1_LDS, stream>>>(x, kc, vc, wq, wo, pws);
  mha_reduce_kernel<<<224, 256, 0, stream>>>(pws, out);
}
```
